```python
import torch

def _softmax(x, axis=-1):
    return torch.softmax(x, dim=axis)

def _gelu(x):
    return torch.nn.functional.gelu(x, approximate='tanh')

def _silu(x):
    return torch.nn.functional.silu(x)

def _sigmoid(x):
    return torch.sigmoid(x)

def _layernorm(x, axis=-1, eps=1e-05):
    m = x.mean(dim=axis, keepdim=True)
    v = x.var(dim=axis, keepdim=True, unbiased=False)
    return (x - m) / torch.sqrt(v + eps)

def _scatter_add(tgt, idx, src, axis=0):
    return tgt.clone().index_add_(axis, idx, src)

def _segment_sum(src, seg, n):
    out = torch.zeros((n,) + src.shape[1:], dtype=src.dtype)
    return out.index_add_(0, seg, src)

def _pad(x, pw):
    flat = tuple((v for pair in reversed(pw) for v in pair))
    return torch.nn.functional.pad(x, flat)
kernel_name = 'parallel_sum__memory_aug_recurrent_mem_tr__glu_gated_glu__prompt'

def setup_inputs():

    _w = lambda a: (lambda t: t.to(torch.bfloat16) if t.is_floating_point() else t)(torch.as_tensor(a))

    def _A_setup():
        B, S, Dm = (1, 2048, 512)
        H, Dh = (32, 128)
        M, seg = (8, 32)
        seg = min(seg, S)
        S = S // seg * seg
        return {'x': _w((lambda *s: torch.randn(*s, dtype=torch.bfloat16) * 0.05)(B, S, Dm)), 'mem0': _w((lambda *s: torch.randn(*s, dtype=torch.bfloat16) * 1.6)(B, M, Dm)), 'Wq': _w((lambda *s: torch.randn(*s, dtype=torch.bfloat16) * 0.8)(Dm, H, Dh) * Dm ** (-0.5)), 'Wk': _w((lambda *s: torch.randn(*s, dtype=torch.bfloat16) * 0.8)(Dm, H, Dh) * Dm ** (-0.5)), 'Wv': _w((lambda *s: torch.randn(*s, dtype=torch.bfloat16) * 1.6)(Dm, H, Dh) * Dm ** (-0.5)), 'Wo': _w((lambda *s: torch.randn(*s, dtype=torch.bfloat16) * 1.6)(H, Dh, Dm) * (H * Dh) ** (-0.5)), 'seg_len': seg}
    _A_in = _A_setup()

    def _B_setup():
        B, S, D, F = (1, 2048, 512, 1792)
        return {'x': _w((lambda *s: torch.randn(*s, dtype=torch.bfloat16) * 0.05)(B, S, D)), 'w_gate': _w((lambda *s: torch.randn(*s, dtype=torch.bfloat16) * 0.05)(D, F)), 'w_up': _w((lambda *s: torch.randn(*s, dtype=torch.bfloat16) * 0.05)(D, F)), 'w_down': _w((lambda *s: torch.randn(*s, dtype=torch.bfloat16) * 0.05)(F, D))}
    _B_in = _B_setup()
    return {'A_x': _A_in['x'], 'A_mem0': _A_in['mem0'], 'A_Wq': _A_in['Wq'], 'A_Wk': _A_in['Wk'], 'A_Wv': _A_in['Wv'], 'A_Wo': _A_in['Wo'], 'A_seg_len': _A_in['seg_len'], 'B_x': _B_in['x'], 'B_w_gate': _B_in['w_gate'], 'B_w_up': _B_in['w_up'], 'B_w_down': _B_in['w_down']}

def reference(A_x, A_mem0, A_Wq, A_Wk, A_Wv, A_Wo, A_seg_len, B_x, B_w_gate, B_w_up, B_w_down):

    def _A_ref(x, mem0, Wq, Wk, Wv, Wo, seg_len):
        B, S, Dm = x.shape
        H, Dh = (Wq.shape[1], Wq.shape[2])
        M = mem0.shape[1]
        scale = Dh ** (-0.5)
        n_seg = S // seg_len
        L = M + seg_len
        idx = torch.arange(L)
        mask = (idx[:, None] < M) | (idx[:, None] >= idx[None, :])
        mem = mem0
        outs = []
        for s in range(n_seg):
            xs = x[:, s * seg_len:(s + 1) * seg_len, :].clone()
            h = torch.concatenate([mem, xs], axis=1)
            q = torch.einsum('btd,dhe->bhte', h, Wq)
            k = torch.einsum('btd,dhe->bhte', h, Wk)
            v = torch.einsum('btd,dhe->bhte', h, Wv)
            scores = torch.einsum('bhqe,bhke->bhqk', q, k) * scale
            scores = torch.where(mask[None, None], scores, -1000000000.0)
            p = _softmax(scores, axis=-1)
            o = torch.einsum('bhqk,bhke->bhqe', p, v)
            o = torch.einsum('bhte,hed->btd', o, Wo)
            mem = o[:, :M, :].clone()
            outs.append(o[:, M:, :])
        return torch.concatenate(outs, axis=1)

    def _B_ref(x, w_gate, w_up, w_down):
        g = torch.einsum('bsd,df->bsf', x, w_gate)
        u = torch.einsum('bsd,df->bsf', x, w_up)
        h = _sigmoid(g) * u
        return torch.einsum('bsf,fd->bsd', h, w_down)
    x = A_x
    xn = _layernorm(x, axis=-1)
    return x + _A_ref(xn, A_mem0, A_Wq, A_Wk, A_Wv, A_Wo, A_seg_len) + _B_ref(xn, B_w_gate, B_w_up, B_w_down)
```

```cpp
#include <hip/hip_runtime.h>
#include <hip/hip_bf16.h>

constexpr int S_ = 2048, DM = 512, NH = 32, DH = 128, MM = 8, SEG = 32, NSEG = 64, FF = 1792;

typedef __bf16 bf16;
typedef __bf16 bf16x8 __attribute__((ext_vector_type(8)));
typedef __bf16 bf16x4 __attribute__((ext_vector_type(4)));
typedef short s16x4 __attribute__((ext_vector_type(4)));
typedef float f32x4 __attribute__((ext_vector_type(4)));
typedef unsigned u32x4 __attribute__((ext_vector_type(4)));
typedef unsigned u32x2 __attribute__((ext_vector_type(2)));
#define LAS __attribute__((address_space(3)))

constexpr size_t OFF_MEMH = 0;
constexpr size_t OFF_PART = 1 << 20;
constexpr size_t OFF_XN   = 4u << 20;
constexpr size_t OFF_BOUT = 6u << 20;
constexpr size_t OFF_H    = 8u << 20;
constexpr size_t OFF_QF   = 16u << 20;
constexpr size_t OFF_KF   = 32u << 20;
constexpr size_t OFF_VF   = 48u << 20;
constexpr size_t OFF_OX   = 64u << 20;
constexpr size_t OFF_KMH  = 80u << 20;
constexpr size_t OFF_VMH  = 88u << 20;

__device__ __forceinline__ f32x4 mfma32(bf16x8 a, bf16x8 b, f32x4 c) {
  return __builtin_amdgcn_mfma_f32_16x16x32_bf16(a, b, c, 0, 0, 0);
}
__device__ __forceinline__ f32x4 mfma16(bf16x4 a, bf16x4 b, f32x4 c) {
  bf16x8 a8, b8;
#pragma unroll
  for (int j = 0; j < 4; ++j) { a8[j] = a[j]; b8[j] = b[j]; a8[4 + j] = (__bf16)0.f; b8[4 + j] = (__bf16)0.f; }
  return __builtin_amdgcn_mfma_f32_16x16x32_bf16(a8, b8, c, 0, 0, 0);
}
__device__ __forceinline__ float bfr(float x) { return (float)(__bf16)x; }
__device__ __forceinline__ bf16x4 tr_read(const char* lds_base, unsigned off) {
  s16x4 t = __builtin_amdgcn_ds_read_tr16_b64_v4i16((LAS s16x4*)(lds_base + off));
  return __builtin_bit_cast(bf16x4, t);
}
__device__ __forceinline__ bf16x8 cat(bf16x4 a, bf16x4 b) {
  bf16x8 r; r[0] = a[0]; r[1] = a[1]; r[2] = a[2]; r[3] = a[3]; r[4] = b[0]; r[5] = b[1]; r[6] = b[2]; r[7] = b[3]; return r;
}
__device__ __forceinline__ bf16x4 pk4(f32x4 v) { bf16x4 r; r[0] = (__bf16)v[0]; r[1] = (__bf16)v[1]; r[2] = (__bf16)v[2]; r[3] = (__bf16)v[3]; return r; }

__global__ __launch_bounds__(256) void k_ln(const bf16* __restrict__ x, const bf16* __restrict__ mem0, char* ws) {
  const int lane = threadIdx.x & 63, row = blockIdx.x * 4 + (threadIdx.x >> 6);
  bf16* xn = (bf16*)(ws + OFF_XN);
  bf16x8 v = *(const bf16x8*)(x + (size_t)row * DM + lane * 8);
  float f[8], s = 0.f;
#pragma unroll
  for (int j = 0; j < 8; ++j) { f[j] = (float)v[j]; s += f[j]; }
#pragma unroll
  for (int m = 32; m >= 1; m >>= 1) s += __shfl_xor(s, m);
  const float mean = s * (1.f / DM);
  float q = 0.f;
#pragma unroll
  for (int j = 0; j < 8; ++j) { float d = f[j] - mean; q += d * d; }
#pragma unroll
  for (int m = 32; m >= 1; m >>= 1) q += __shfl_xor(q, m);
  const float mb = bfr(mean), vb = bfr(q * (1.f / DM));
  const float den = bfr(sqrtf(bfr(vb + 1e-5f)));
  bf16x8 o;
#pragma unroll
  for (int j = 0; j < 8; ++j) o[j] = (__bf16)(bfr(f[j] - mb) / den);
  *(bf16x8*)(xn + (size_t)row * DM + lane * 8) = o;
  if (blockIdx.x == 0) {
    for (int c = threadIdx.x; c < 512; c += 256) {
      const int i = c & 7, dg = c >> 3;
      *(bf16x8*)(ws + OFF_MEMH + (size_t)c * 16) = *(const bf16x8*)(mem0 + i * DM + dg * 8);
    }
  }
}

constexpr int GA_PITCH = 144;
constexpr int GA_BYTES = 128 * GA_PITCH;
constexpr int GB_BYTES = 64 * 256;
constexpr int GEMM_LDS = GA_BYTES + GB_BYTES;
__device__ __forceinline__ unsigned off_b(unsigned row, unsigned ch) { return 256u * row + 16u * (ch ^ (((row & 3) << 2) | ((row >> 2) & 3))); }

template <int MODE>
__device__ __forceinline__ void gemm_tile(const bf16* __restrict__ A, int lda, const bf16* __restrict__ B0, const bf16* __restrict__ B1,
                                          int ldb, int K, int row0, int col0, char* lds, char* ws, const bf16* __restrict__ xin,
                                          bf16* __restrict__ out, bf16* dstbase) {
  const int tid = threadIdx.x, lane = tid & 63, w = tid >> 6, wm = w >> 1, wn = w & 1;
  const int li = lane & 15, g = lane >> 4, q = li >> 2, p = li & 3;
  char* la = lds; char* lb = lds + GA_BYTES;
  f32x4 acc[2][4];
#pragma unroll
  for (int a = 0; a < 2; ++a)
#pragma unroll
    for (int b = 0; b < 4; ++b) acc[a][b] = (f32x4){0.f, 0.f, 0.f, 0.f};
  const bf16* asrc[2]; const bf16* bsrc[2]; unsigned adst[2], bdst[2];
#pragma unroll
  for (int u = 0; u < 2; ++u) {
    const int c = tid + 512 * u;
    asrc[u] = A + (size_t)(row0 + (c >> 3)) * lda + (c & 7) * 8;
    adst[u] = (c >> 3) * GA_PITCH + (c & 7) * 16;
    const int kr = c >> 4, ch = c & 15;
    if (MODE == 2) {
      const int blk = ch >> 2;
      bsrc[u] = ((blk & 1) ? B1 : B0) + (size_t)kr * ldb + col0 + (blk >> 1) * 32 + (ch & 3) * 8;
    } else {
      bsrc[u] = B0 + (size_t)kr * ldb + col0 + ch * 8;
    }
    bdst[u] = off_b(kr, ch);
  }
  u32x4 ra[2], rb[2];
#pragma unroll
  for (int u = 0; u < 2; ++u) { ra[u] = *(const u32x4*)asrc[u]; rb[u] = *(const u32x4*)bsrc[u]; }
  const int nkt = K >> 6;
  for (int kt = 0; kt < nkt; ++kt) {
#pragma unroll
    for (int u = 0; u < 2; ++u) { *(u32x4*)(la + adst[u]) = ra[u]; *(u32x4*)(lb + bdst[u]) = rb[u]; }
    __syncthreads();
    if (kt + 1 < nkt) {
#pragma unroll
      for (int u = 0; u < 2; ++u) {
        ra[u] = *(const u32x4*)(asrc[u] + (kt + 1) * 64);
        rb[u] = *(const u32x4*)(bsrc[u] + (size_t)(kt + 1) * 64 * ldb);
      }
    }
#pragma unroll
    for (int ks = 0; ks < 2; ++ks) {
      bf16x8 af[2], bfr_[4];
#pragma unroll
      for (int tt = 0; tt < 2; ++tt) af[tt] = *(const bf16x8*)(la + (wm * 32 + tt * 16 + li) * GA_PITCH + ks * 64 + g * 16);
#pragma unroll
      for (int nt = 0; nt < 4; ++nt) {
        const unsigned ch = 2 * (wn * 4 + nt) + (p >> 1);
        bf16x4 lo = tr_read(lb, off_b(32 * ks + 8 * g + q, ch) + 8 * (p & 1));
        bf16x4 hi = tr_read(lb, off_b(32 * ks + 8 * g + 4 + q, ch) + 8 * (p & 1));
        bfr_[nt] = cat(lo, hi);
      }
#pragma unroll
      for (int tt = 0; tt < 2; ++tt)
#pragma unroll
        for (int nt = 0; nt < 4; ++nt) {
          if (MODE == 1) acc[tt][nt] = mfma32(af[tt], bfr_[nt], acc[tt][nt]);
          else acc[tt][nt] = mfma32(bfr_[nt], af[tt], acc[tt][nt]);
        }
    }
    __syncthreads();
  }
  if (MODE == 0) {
    const int seg = (row0 >> 5) + wm, head = col0 >> 7;
#pragma unroll
    for (int tt = 0; tt < 2; ++tt)
#pragma unroll
      for (int k2 = 0; k2 < 2; ++k2) {
        bf16x8 o = cat(pk4(acc[tt][2 * k2]), pk4(acc[tt][2 * k2 + 1]));
        *(bf16x8*)(dstbase + ((((size_t)seg * NH + head) * 2 + tt) * 4 + (wn * 2 + k2)) * 512 + lane * 8) = o;
      }
  } else if (MODE == 1) {
    const int seg = (row0 >> 5) + wm, head = col0 >> 7;
#pragma unroll
    for (int nt = 0; nt < 4; ++nt) {
      bf16x8 o = cat(pk4(acc[0][nt]), pk4(acc[1][nt]));
      *(bf16x8*)(dstbase + (((size_t)seg * NH + head) * 8 + (wn * 4 + nt)) * 512 + lane * 8) = o;
    }
  } else if (MODE == 2) {
#pragma unroll
    for (int tt = 0; tt < 2; ++tt)
#pragma unroll
      for (int n2 = 0; n2 < 2; ++n2) {
        f32x4 hv;
#pragma unroll
        for (int r = 0; r < 4; ++r) {
          const float gg = bfr(acc[tt][n2][r]), uu = bfr(acc[tt][n2 + 2][r]);
          const float sg = bfr(1.f / (1.f + __expf(-gg)));
          hv[r] = sg * uu;
        }
        const int row = row0 + wm * 32 + tt * 16 + li, col = col0 + 32 * wn + 16 * n2 + 4 * g;
        *(bf16x4*)(dstbase + (size_t)row * FF + col) = pk4(hv);
      }
  } else if (MODE == 3) {
#pragma unroll
    for (int tt = 0; tt < 2; ++tt)
#pragma unroll
      for (int nt = 0; nt < 4; ++nt) {
        const int row = row0 + wm * 32 + tt * 16 + li, col = col0 + 64 * wn + 16 * nt + 4 * g;
        *(bf16x4*)(dstbase + (size_t)row * DM + col) = pk4(acc[tt][nt]);
      }
  } else {
    const bf16* bout = (const bf16*)(ws + OFF_BOUT);
#pragma unroll
    for (int tt = 0; tt < 2; ++tt)
#pragma unroll
      for (int nt = 0; nt < 4; ++nt) {
        const int row = row0 + wm * 32 + tt * 16 + li, col = col0 + 64 * wn + 16 * nt + 4 * g;
        const bf16x4 xv = *(const bf16x4*)(xin + (size_t)row * DM + col);
        const bf16x4 bv = *(const bf16x4*)(bout + (size_t)row * DM + col);
        f32x4 o;
#pragma unroll
        for (int r = 0; r < 4; ++r) o[r] = bfr((float)xv[r] + bfr(acc[tt][nt][r])) + (float)bv[r];
        *(bf16x4*)(out + (size_t)row * DM + col) = pk4(o);
      }
  }
}

__global__ __launch_bounds__(512, 2) void k_proj(const bf16* Wq, const bf16* Wk, const bf16* Wv, const bf16* Wg, const bf16* Wu, char* ws) {
  extern __shared__ __attribute__((aligned(16))) char lds[];
  const int cb = blockIdx.x, row0 = blockIdx.y * 128;
  const bf16* xn = (const bf16*)(ws + OFF_XN);
  if (cb < 32) gemm_tile<0>(xn, DM, Wq, nullptr, NH * DH, DM, row0, cb * 128, lds, ws, nullptr, nullptr, (bf16*)(ws + OFF_QF));
  else if (cb < 64) gemm_tile<0>(xn, DM, Wk, nullptr, NH * DH, DM, row0, (cb - 32) * 128, lds, ws, nullptr, nullptr, (bf16*)(ws + OFF_KF));
  else if (cb < 96) gemm_tile<1>(xn, DM, Wv, nullptr, NH * DH, DM, row0, (cb - 64) * 128, lds, ws, nullptr, nullptr, (bf16*)(ws + OFF_VF));
  else gemm_tile<2>(xn, DM, Wg, Wu, FF, DM, row0, (cb - 96) * 64, lds, ws, nullptr, nullptr, (bf16*)(ws + OFF_H));
}
__global__ __launch_bounds__(512, 2) void k_bout(const bf16* Wd, char* ws) {
  extern __shared__ __attribute__((aligned(16))) char lds[];
  gemm_tile<3>((const bf16*)(ws + OFF_H), FF, Wd, nullptr, DM, FF, blockIdx.y * 128, blockIdx.x * 128, lds, ws, nullptr, nullptr, (bf16*)(ws + OFF_BOUT));
}
__global__ __launch_bounds__(512, 2) void k_final(const bf16* Wo, const bf16* x, bf16* out, char* ws) {
  extern __shared__ __attribute__((aligned(16))) char lds[];
  gemm_tile<4>((const bf16*)(ws + OFF_OX), NH * DH, Wo, nullptr, DM, NH * DH, blockIdx.y * 128, blockIdx.x * 128, lds, ws, x, out, nullptr);
}

__global__ __launch_bounds__(256) void k_xattn(char* ws) {
  const int lane = threadIdx.x & 63, li = lane & 15, g = lane >> 4;
  const int unit = blockIdx.x * 4 + (threadIdx.x >> 6);
  const int seg = unit >> 5, head = unit & 31;
  const bf16* qf = (const bf16*)(ws + OFF_QF) + (size_t)unit * 4096;
  const bf16* kf = (const bf16*)(ws + OFF_KF) + (size_t)unit * 4096;
  const bf16* vf = (const bf16*)(ws + OFF_VF) + (size_t)unit * 4096;
  const bf16* km = (const bf16*)(ws + OFF_KMH) + (size_t)unit * 2048;
  const bf16* vm = (const bf16*)(ws + OFF_VMH) + (size_t)unit * 2048;
  bf16* ox = (bf16*)(ws + OFF_OX);
  const float scale = 0.08838834764831845f;
  f32x4 sc[3][2];
#pragma unroll
  for (int a = 0; a < 3; ++a) { sc[a][0] = (f32x4){0, 0, 0, 0}; sc[a][1] = (f32x4){0, 0, 0, 0}; }
#pragma unroll
  for (int kk = 0; kk < 4; ++kk) {
    const bf16x8 q0 = *(const bf16x8*)(qf + (0 * 4 + kk) * 512 + lane * 8), q1 = *(const bf16x8*)(qf + (1 * 4 + kk) * 512 + lane * 8);
    const bf16x8 k0 = *(const bf16x8*)(km + kk * 512 + lane * 8);
    const bf16x8 k1 = *(const bf16x8*)(kf + (0 * 4 + kk) * 512 + lane * 8), k2 = *(const bf16x8*)(kf + (1 * 4 + kk) * 512 + lane * 8);
    sc[0][0] = mfma32(k0, q0, sc[0][0]); sc[0][1] = mfma32(k0, q1, sc[0][1]);
    sc[1][0] = mfma32(k1, q0, sc[1][0]); sc[1][1] = mfma32(k1, q1, sc[1][1]);
    sc[2][0] = mfma32(k2, q0, sc[2][0]); sc[2][1] = mfma32(k2, q1, sc[2][1]);
  }
  bf16x4 p0[2]; bf16x8 p12[2];
#pragma unroll
  for (int qt = 0; qt < 2; ++qt) {
    const int qi = 16 * qt + li;
    float v[12]; float m = -3.0e38f;
#pragma unroll
    for (int kt = 0; kt < 3; ++kt)
#pragma unroll
      for (int r = 0; r < 4; ++r) {
        float s = bfr(bfr(sc[kt][qt][r]) * scale);
        bool ok = (kt == 0) ? (g < 2) : (16 * (kt - 1) + 4 * g + r <= qi);
        s = ok ? s : -3.0e38f;
        v[kt * 4 + r] = s; m = fmaxf(m, s);
      }
    m = fmaxf(m, __shfl_xor(m, 16)); m = fmaxf(m, __shfl_xor(m, 32));
    float sum = 0.f;
#pragma unroll
    for (int e = 0; e < 12; ++e) { v[e] = (v[e] > -1.0e38f) ? __expf(v[e] - m) : 0.f; sum += v[e]; }
    sum += __shfl_xor(sum, 16); sum += __shfl_xor(sum, 32);
    const float inv = 1.f / sum;
#pragma unroll
    for (int r = 0; r < 4; ++r) { p0[qt][r] = (__bf16)(v[r] * inv); p12[qt][r] = (__bf16)(v[4 + r] * inv); p12[qt][4 + r] = (__bf16)(v[8 + r] * inv); }
  }
#pragma unroll
  for (int et = 0; et < 8; ++et) {
    const bf16x4 vmf = *(const bf16x4*)(vm + et * 256 + lane * 4);
    const bf16x8 vxf = *(const bf16x8*)(vf + et * 512 + lane * 8);
#pragma unroll
    for (int qt = 0; qt < 2; ++qt) {
      f32x4 o = mfma16(vmf, p0[qt], (f32x4){0, 0, 0, 0});
      o = mfma32(vxf, p12[qt], o);
      *(bf16x4*)(ox + (size_t)(seg * 32 + 16 * qt + li) * (NH * DH) + head * DH + 16 * et + 4 * g) = pk4(o);
    }
  }
}

constexpr int CL_SCR = 0;
constexpr int CL_KX = 0;
constexpr int CL_VX = CL_KX + 8192;
constexpr int CL_MEM = CL_VX + 4096;
constexpr int CL_QF = CL_MEM + 8192;
constexpr int CL_KF = CL_QF + 4096;
constexpr int CL_OF = CL_KF + 4096;
constexpr int CL_WV = 73728;
constexpr int CHAIN_LDS = CL_WV + 65536;
constexpr unsigned SENT = 0xFFFFFFFFu;
constexpr unsigned SPIN_CAP = 1u << 22;

__global__ __launch_bounds__(512, 2) void k_chain(const bf16* __restrict__ Wq, const bf16* __restrict__ Wk, const bf16* __restrict__ Wv,
                                                  const bf16* __restrict__ Wo, char* ws) {
  extern __shared__ __attribute__((aligned(16))) char lds[];
  const int tid = threadIdx.x, lane = tid & 63, w = __builtin_amdgcn_readfirstlane(tid >> 6);
  const int li = lane & 15, g = lane >> 4, q4 = li >> 2, p4 = li & 3;
  const int b = blockIdx.x, h = b >> 1, c = b & 1;
  char* scr = lds + CL_SCR;
  bf16x8 wqk[2][16];
  bf16x8 wo[4][2];
  {
    constexpr int P1 = 272;
#pragma unroll
    for (int it = 0; it < 4; ++it) {
#pragma unroll
      for (int u = 0; u < 8; ++u) {
        const int cidx = tid + 512 * u;
        const int mat = cidx >> 11, rr = (cidx >> 4) & 127, ch = cidx & 15;
        const bf16* src = (mat ? Wk : Wq) + (size_t)(128 * it + rr) * (NH * DH) + h * DH + ch * 8;
        *(u32x4*)(scr + mat * (128 * P1) + rr * P1 + ch * 16) = *(const u32x4*)src;
      }
      __syncthreads();
      const char* base = scr + (w >> 2) * (128 * P1);
#pragma unroll
      for (int k4 = 0; k4 < 4; ++k4)
#pragma unroll
        for (int t = 0; t < 2; ++t) {
          const int c16 = 2 * (w & 3) + t;
          bf16x4 lo = tr_read(base, (32 * k4 + 8 * g + q4) * P1 + (16 * c16 + 4 * p4) * 2);
          bf16x4 hi = tr_read(base, (32 * k4 + 8 * g + 4 + q4) * P1 + (16 * c16 + 4 * p4) * 2);
          wqk[t][4 * it + k4] = cat(lo, hi);
        }
      __syncthreads();
    }
    constexpr int P2 = 1040;
#pragma unroll
    for (int u = 0; u < 8; ++u) {
      const int cidx = tid + 512 * u;
      const int rr = cidx >> 6, ch = cidx & 63;
      *(u32x4*)(scr + rr * P2 + ch * 16) = *(const u32x4*)(Wo + ((size_t)h * DH + c * 64 + rr) * DM + ch * 8);
    }
    __syncthreads();
#pragma unroll
    for (int nt = 0; nt < 4; ++nt)
#pragma unroll
      for (int k2 = 0; k2 < 2; ++k2) {
        const int col = 64 * w + 16 * nt + 4 * p4;
        bf16x4 lo = tr_read(scr, ((2 * k2 + 0) * 16 + 4 * g + q4) * P2 + col * 2);
        bf16x4 hi = tr_read(scr, ((2 * k2 + 1) * 16 + 4 * g + q4) * P2 + col * 2);
        wo[nt][k2] = cat(lo, hi);
      }
    __syncthreads();
    constexpr int P3 = 144;
#pragma unroll
    for (int u = 0; u < 8; ++u) {
      const int cidx = tid + 512 * u;
      const int rr = cidx >> 3, ch = cidx & 7;
      *(u32x4*)(scr + rr * P3 + ch * 16) = *(const u32x4*)(Wv + (size_t)rr * (NH * DH) + h * DH + c * 64 + ch * 8);
    }
    __syncthreads();
#pragma unroll
    for (int et = 0; et < 4; ++et)
#pragma unroll
      for (int k2 = 0; k2 < 2; ++k2) {
        const int ks = 2 * w + k2;
        bf16x4 lo = tr_read(scr, (32 * ks + 8 * g + q4) * P3 + (16 * et + 4 * p4) * 2);
        bf16x4 hi = tr_read(scr, (32 * ks + 8 * g + 4 + q4) * P3 + (16 * et + 4 * p4) * 2);
        *(bf16x8*)(lds + CL_WV + ((et * 16 + ks) * 64 + lane) * 16) = cat(lo, hi);
      }
    __syncthreads();
  }
  const auto rs_mem = __builtin_amdgcn_make_buffer_rsrc(ws + OFF_MEMH, 0, 65 * 8192, 0x00020000);
  const auto rs_part = __builtin_amdgcn_make_buffer_rsrc(ws + OFF_PART, 0, 2 << 20, 0x00020000);
  const bf16* kfg = (const bf16*)(ws + OFF_KF);
  const bf16* vfg = (const bf16*)(ws + OFF_VF);
  bf16* kmh = (bf16*)(ws + OFF_KMH);
  bf16* vmh = (bf16*)(ws + OFF_VMH);
  const float scale = 0.08838834764831845f;
  {
    if (w < 6) {
#pragma unroll
      for (int u = 0; u < 2; ++u) {
        const int cidx = tid + 384 * u;
        const u32x4 v = (cidx < 512) ? *(const u32x4*)(kfg + (size_t)h * 4096 + cidx * 8)
                                     : *(const u32x4*)(vfg + (size_t)h * 4096 + c * 2048 + (cidx - 512) * 8);
        *(u32x4*)(lds + CL_KX + cidx * 16) = v;
      }
    }
  }
  for (int s = 0; s < NSEG; ++s) {
    {
      u32x4 v; unsigned spins = 0;
      for (;;) {
        asm volatile("" ::: "memory");
        v = __builtin_amdgcn_raw_buffer_load_b128(rs_mem, s * 8192 + tid * 16, 0, 16);
        const bool ok = (v[0] != SENT) & (v[1] != SENT) & (v[2] != SENT) & (v[3] != SENT);
        if (__all(ok)) break;
        if (++spins > SPIN_CAP) break;
      }
      *(u32x4*)(lds + CL_MEM + tid * 16) = v;
    }
    __syncthreads();
    u32x4 pf[2];
    const int sn = (s + 1 < NSEG) ? s + 1 : s;
    if (w < 6) {
#pragma unroll
      for (int u = 0; u < 2; ++u) {
        const int cidx = tid + 384 * u;
        pf[u] = (cidx < 512) ? *(const u32x4*)(kfg + ((size_t)sn * NH + h) * 4096 + cidx * 8)
                             : *(const u32x4*)(vfg + ((size_t)sn * NH + h) * 4096 + c * 2048 + (cidx - 512) * 8);
      }
    }
    {
      f32x4 a0 = {0, 0, 0, 0}, a1 = {0, 0, 0, 0};
#pragma unroll
      for (int ks = 0; ks < 16; ++ks) {
        const bf16x8 mf = *(const bf16x8*)(lds + CL_MEM + ((ks * 4 + g) * 8 + (li & 7)) * 16);
        a0 = mfma32(wqk[0][ks], mf, a0);
        a1 = mfma32(wqk[1][ks], mf, a1);
      }
      const bf16x8 o = cat(pk4(a0), pk4(a1));
      *(bf16x8*)(lds + (w < 4 ? CL_QF : CL_KF) + ((w & 3) * 64 + lane) * 16) = o;
      if (w >= 4 && c == 0) *(bf16x8*)(kmh + (((size_t)s * NH + h) * 4 + (w & 3)) * 512 + lane * 8) = o;
    }
    __syncthreads();
    if (w < 4) {
      f32x4 va = {0, 0, 0, 0};
      f32x4 s0 = {0, 0, 0, 0}, s1 = {0, 0, 0, 0}, s2 = {0, 0, 0, 0};
#pragma unroll
      for (int kk = 0; kk < 4; ++kk) {
        const bf16x8 qf = *(const bf16x8*)(lds + CL_QF + (kk * 64 + lane) * 16);
        const bf16x8 k0 = *(const bf16x8*)(lds + CL_KF + (kk * 64 + lane) * 16);
        const bf16x8 k1 = *(const bf16x8*)(lds + CL_KX + ((0 * 4 + kk) * 64 + lane) * 16);
        const bf16x8 k2 = *(const bf16x8*)(lds + CL_KX + ((1 * 4 + kk) * 64 + lane) * 16);
        s0 = mfma32(k0, qf, s0); s1 = mfma32(k1, qf, s1); s2 = mfma32(k2, qf, s2);
        if (kk & 1) __builtin_amdgcn_sched_barrier(0);
      }
#pragma unroll
      for (int k4 = 0; k4 < 4; ++k4) {
#pragma unroll
        for (int k1 = 0; k1 < 4; ++k1) {
          const int ks = 4 * k4 + k1;
          const bf16x8 mf = *(const bf16x8*)(lds + CL_MEM + ((ks * 4 + g) * 8 + (li & 7)) * 16);
          const bf16x8 wv = *(const bf16x8*)(lds + CL_WV + ((w * 16 + ks) * 64 + lane) * 16);
          va = mfma32(mf, wv, va);
        }
        __builtin_amdgcn_sched_barrier(0);
      }
      float v[12]; float m = -3.0e38f;
#pragma unroll
      for (int r = 0; r < 4; ++r) {
        v[r] = (g < 2) ? bfr(bfr(s0[r]) * scale) : -3.0e38f;
        v[4 + r] = bfr(bfr(s1[r]) * scale);
        v[8 + r] = bfr(bfr(s2[r]) * scale);
      }
#pragma unroll
      for (int e = 0; e < 12; ++e) m = fmaxf(m, v[e]);
      m = fmaxf(m, __shfl_xor(m, 16)); m = fmaxf(m, __shfl_xor(m, 32));
      float sum = 0.f;
#pragma unroll
      for (int e = 0; e < 12; ++e) { v[e] = (v[e] > -1.0e38f) ? __expf(v[e] - m) : 0.f; sum += v[e]; }
      sum += __shfl_xor(sum, 16); sum += __shfl_xor(sum, 32);
      const float inv = 1.f / sum;
      bf16x4 p0; bf16x8 p12;
#pragma unroll
      for (int r = 0; r < 4; ++r) { p0[r] = (__bf16)(v[r] * inv); p12[r] = (__bf16)(v[4 + r] * inv); p12[4 + r] = (__bf16)(v[8 + r] * inv); }
      const bf16x4 vm4 = pk4(va);
      const bf16x8 vx8 = *(const bf16x8*)(lds + CL_VX + (w * 64 + lane) * 16);
      f32x4 o = mfma16(vm4, p0, (f32x4){0, 0, 0, 0});
      o = mfma32(vx8, p12, o);
      *(bf16x4*)(lds + CL_OF + ((w >> 1) * 64 + lane) * 16 + (w & 1) * 8) = pk4(o);
      *(bf16x4*)(vmh + (((size_t)s * NH + h) * 8 + 4 * c + w) * 256 + lane * 4) = vm4;
    }
    __syncthreads();
    if (s + 1 == NSEG) break;
    const unsigned tag = ((s >> 1) + 1) & 3u;
    const int slot = (s & 1) << 20;
    {
      const bf16x8 of0 = *(const bf16x8*)(lds + CL_OF + (0 * 64 + lane) * 16);
      const bf16x8 of1 = *(const bf16x8*)(lds + CL_OF + (1 * 64 + lane) * 16);
#pragma unroll
      for (int nt = 0; nt < 4; ++nt) {
        f32x4 pa = mfma32(wo[nt][0], of0, (f32x4){0, 0, 0, 0});
        pa = mfma32(wo[nt][1], of1, pa);
        u32x4 pv = __builtin_bit_cast(u32x4, pa);
#pragma unroll
        for (int r = 0; r < 4; ++r) pv[r] = (pv[r] & ~3u) | tag;
        const int j2 = 16 * w + 4 * nt + g;
        if (li < 8) __builtin_amdgcn_raw_buffer_store_b128(pv, rs_part, slot + ((j2 * 64 + b) * 8 + li) * 16, 0, 16);
      }
    }
    if (w < 6) {
#pragma unroll
      for (int u = 0; u < 2; ++u) *(u32x4*)(lds + CL_KX + (tid + 384 * u) * 16) = pf[u];
    }
    if (w >= 6) {
      const int j2 = 2 * b + (w - 6);
      u32x4 v[8]; unsigned spins = 0;
      for (;;) {
        asm volatile("" ::: "memory");
        bool ok = true;
#pragma unroll
        for (int it = 0; it < 8; ++it) v[it] = __builtin_amdgcn_raw_buffer_load_b128(rs_part, slot + (j2 * 512 + it * 64 + lane) * 16, 0, 16);
#pragma unroll
        for (int it = 0; it < 8; ++it)
          ok &= (((v[it][0] & 3u) == tag) & ((v[it][1] & 3u) == tag) & ((v[it][2] & 3u) == tag) & ((v[it][3] & 3u) == tag));
        if (__all(ok)) break;
        if (++spins > SPIN_CAP) break;
      }
      f32x4 a = {0, 0, 0, 0};
#pragma unroll
      for (int it = 0; it < 8; ++it) a += __builtin_bit_cast(f32x4, v[it]);
#pragma unroll
      for (int r = 0; r < 4; ++r) { a[r] += __shfl_xor(a[r], 8); a[r] += __shfl_xor(a[r], 16); a[r] += __shfl_xor(a[r], 32); }
      u32x2 o = __builtin_bit_cast(u32x2, pk4(a));
      if (o[0] == SENT) o[0] = 0x7FFF7FFFu;
      if (o[1] == SENT) o[1] = 0x7FFF7FFFu;
      if (lane < 8) __builtin_amdgcn_raw_buffer_store_b64(o, rs_mem, (s + 1) * 8192 + b * 128 + lane * 16 + (w - 6) * 8, 0, 16);
    }
  }
}

extern "C" void kernel_launch(void* const* d_in, const int* in_sizes, int n_in,
                              void* d_out, int out_size, void* d_ws, size_t ws_size,
                              hipStream_t stream) {
  const bf16* x = (const bf16*)d_in[0];
  const bf16* mem0 = (const bf16*)d_in[1];
  const bf16* Wq = (const bf16*)d_in[2];
  const bf16* Wk = (const bf16*)d_in[3];
  const bf16* Wv = (const bf16*)d_in[4];
  const bf16* Wo = (const bf16*)d_in[5];
  const bf16* Wg = (const bf16*)d_in[8];
  const bf16* Wu = (const bf16*)d_in[9];
  const bf16* Wd = (const bf16*)d_in[10];
  char* ws = (char*)d_ws;
  hipFuncSetAttribute((const void*)k_chain, hipFuncAttributeMaxDynamicSharedMemorySize, CHAIN_LDS);
  hipMemsetAsync(ws + OFF_MEMH, 0xFF, 65 * 8192, stream);
  hipMemsetAsync(ws + OFF_PART, 0, 2 << 20, stream);
  k_ln<<<S_ / 4, 256, 0, stream>>>(x, mem0, ws);
  k_proj<<<dim3(124, 16), 512, GEMM_LDS, stream>>>(Wq, Wk, Wv, Wg, Wu, ws);
  k_chain<<<64, 512, CHAIN_LDS, stream>>>(Wq, Wk, Wv, Wo, ws);
  k_bout<<<dim3(4, 16), 512, GEMM_LDS, stream>>>(Wd, ws);
  k_xattn<<<NSEG * NH / 4, 256, 0, stream>>>(ws);
  k_final<<<dim3(4, 16), 512, GEMM_LDS, stream>>>(Wo, x, (bf16*)d_out, ws);
}
```

```cpp
#include <hip/hip_runtime.h>
#include <hip/hip_bf16.h>

constexpr int S_ = 2048, DM = 512, NH = 32, DH = 128, MM = 8, SEG = 32, NSEG = 64, FF = 1792;

typedef __bf16 bf16;
typedef __bf16 bf16x8 __attribute__((ext_vector_type(8)));
typedef __bf16 bf16x4 __attribute__((ext_vector_type(4)));
typedef short s16x4 __attribute__((ext_vector_type(4)));
typedef float f32x4 __attribute__((ext_vector_type(4)));
typedef unsigned u32x4 __attribute__((ext_vector_type(4)));
typedef unsigned u32x2 __attribute__((ext_vector_type(2)));
#define LAS __attribute__((address_space(3)))

constexpr size_t OFF_MEMH = 0;
constexpr size_t OFF_PART = 1 << 20;
constexpr size_t OFF_CNT  = 2 << 20;
constexpr size_t OFF_XN   = 4u << 20;
constexpr size_t OFF_BOUT = 6u << 20;
constexpr size_t OFF_H    = 8u << 20;
constexpr size_t OFF_QF   = 16u << 20;
constexpr size_t OFF_KF   = 32u << 20;
constexpr size_t OFF_VF   = 48u << 20;
constexpr size_t OFF_OX   = 64u << 20;
constexpr size_t OFF_KMH  = 80u << 20;
constexpr size_t OFF_VMH  = 88u << 20;
constexpr size_t OFF_WOF  = 96u << 20;
constexpr unsigned WS_SPAN = 100u << 20;
constexpr int C_TICKET = 0, C_KV = 1, C_Q = 17, C_H = 33, C_BO = 49, C_WOF = 65, C_X = 66, C_CH = 130;
#define RLX_AGENT __ATOMIC_RELAXED, __HIP_MEMORY_SCOPE_AGENT

__device__ __forceinline__ f32x4 mfma32(bf16x8 a, bf16x8 b, f32x4 c) {
  return __builtin_amdgcn_mfma_f32_16x16x32_bf16(a, b, c, 0, 0, 0);
}
__device__ __forceinline__ f32x4 mfma16(bf16x4 a, bf16x4 b, f32x4 c) {
  bf16x8 a8, b8;
#pragma unroll
  for (int j = 0; j < 4; ++j) { a8[j] = a[j]; b8[j] = b[j]; a8[4 + j] = (__bf16)0.f; b8[4 + j] = (__bf16)0.f; }
  return __builtin_amdgcn_mfma_f32_16x16x32_bf16(a8, b8, c, 0, 0, 0);
}
__device__ __forceinline__ float bfr(float x) { return (float)(__bf16)x; }
__device__ __forceinline__ bf16x4 tr_read(const char* lds_base, unsigned off) {
  s16x4 t = __builtin_amdgcn_ds_read_tr16_b64_v4i16((LAS s16x4*)(lds_base + off));
  return __builtin_bit_cast(bf16x4, t);
}
__device__ __forceinline__ bf16x8 cat(bf16x4 a, bf16x4 b) {
  bf16x8 r; r[0] = a[0]; r[1] = a[1]; r[2] = a[2]; r[3] = a[3]; r[4] = b[0]; r[5] = b[1]; r[6] = b[2]; r[7] = b[3]; return r;
}
__device__ __forceinline__ bf16x4 pk4(f32x4 v) { bf16x4 r; r[0] = (__bf16)v[0]; r[1] = (__bf16)v[1]; r[2] = (__bf16)v[2]; r[3] = (__bf16)v[3]; return r; }

__device__ __forceinline__ float xmax16_32(float x) {
  unsigned u = __builtin_bit_cast(unsigned, x);
  auto a = __builtin_amdgcn_permlane16_swap(u, u, false, false);
  x = fmaxf(__builtin_bit_cast(float, (unsigned)a[0]), __builtin_bit_cast(float, (unsigned)a[1]));
  u = __builtin_bit_cast(unsigned, x);
  auto c = __builtin_amdgcn_permlane32_swap(u, u, false, false);
  return fmaxf(__builtin_bit_cast(float, (unsigned)c[0]), __builtin_bit_cast(float, (unsigned)c[1]));
}
__device__ __forceinline__ float xsum8_16_32(float x) {
  x += __builtin_bit_cast(float, __builtin_amdgcn_update_dpp(0, __builtin_bit_cast(int, x), 0x128, 0xf, 0xf, false));
  unsigned u = __builtin_bit_cast(unsigned, x);
  auto a = __builtin_amdgcn_permlane16_swap(u, u, false, false);
  x = __builtin_bit_cast(float, (unsigned)a[0]) + __builtin_bit_cast(float, (unsigned)a[1]);
  u = __builtin_bit_cast(unsigned, x);
  auto c = __builtin_amdgcn_permlane32_swap(u, u, false, false);
  return __builtin_bit_cast(float, (unsigned)c[0]) + __builtin_bit_cast(float, (unsigned)c[1]);
}

__global__ __launch_bounds__(256) void k_ln(const bf16* __restrict__ x, const bf16* __restrict__ mem0, char* ws) {
  const int lane = threadIdx.x & 63, row = blockIdx.x * 4 + (threadIdx.x >> 6);
  bf16* xn = (bf16*)(ws + OFF_XN);
  bf16x8 v = *(const bf16x8*)(x + (size_t)row * DM + lane * 8);
  float f[8], s = 0.f;
#pragma unroll
  for (int j = 0; j < 8; ++j) { f[j] = (float)v[j]; s += f[j]; }
#pragma unroll
  for (int m = 32; m >= 1; m >>= 1) s += __shfl_xor(s, m);
  const float mean = s * (1.f / DM);
  float q = 0.f;
#pragma unroll
  for (int j = 0; j < 8; ++j) { float d = f[j] - mean; q += d * d; }
#pragma unroll
  for (int m = 32; m >= 1; m >>= 1) q += __shfl_xor(q, m);
  const float mb = bfr(mean), vb = bfr(q * (1.f / DM));
  const float den = bfr(sqrtf(bfr(vb + 1e-5f)));
  bf16x8 o;
#pragma unroll
  for (int j = 0; j < 8; ++j) o[j] = (__bf16)(bfr(f[j] - mb) / den);
  *(bf16x8*)(xn + (size_t)row * DM + lane * 8) = o;
  if (blockIdx.x == 0) {
    for (int c = threadIdx.x; c < 512; c += 256) {
      const int i = c & 7, dg = c >> 3;
      *(bf16x8*)(ws + OFF_MEMH + (size_t)c * 16) = *(const bf16x8*)(mem0 + i * DM + dg * 8);
    }
  }
}

constexpr int GA_PITCH = 144;
constexpr int GA_BYTES = 128 * GA_PITCH;
constexpr int GB_BYTES = 64 * 256;
__device__ __forceinline__ unsigned off_b(unsigned row, unsigned ch) { return 256u * row + 16u * (ch ^ (((row & 3) << 2) | ((row >> 2) & 3))); }
typedef __amdgpu_buffer_rsrc_t rsrc_t;
__device__ __forceinline__ void st16(rsrc_t rs, unsigned off, bf16x8 v) { __builtin_amdgcn_raw_buffer_store_b128(__builtin_bit_cast(u32x4, v), rs, off, 0, 16); }
__device__ __forceinline__ void st8(rsrc_t rs, unsigned off, bf16x4 v) { __builtin_amdgcn_raw_buffer_store_b64(__builtin_bit_cast(u32x2, v), rs, off, 0, 16); }

template <int MODE>
__device__ __forceinline__ void gemm_tile(const bf16* __restrict__ A, int lda, const bf16* __restrict__ B0, const bf16* __restrict__ B1,
                                          int ldb, int K, int row0, int col0, char* lds, rsrc_t rs, unsigned dst) {
  const int tid = threadIdx.x, lane = tid & 63, w = tid >> 6, wm = w >> 1, wn = w & 1;
  const int li = lane & 15, g = lane >> 4, q = li >> 2, p = li & 3;
  char* la = lds; char* lb = lds + GA_BYTES;
  f32x4 acc[2][4];
#pragma unroll
  for (int a = 0; a < 2; ++a)
#pragma unroll
    for (int b = 0; b < 4; ++b) acc[a][b] = (f32x4){0.f, 0.f, 0.f, 0.f};
  const bf16* asrc[2]; const bf16* bsrc[2]; unsigned adst[2], bdst[2];
#pragma unroll
  for (int u = 0; u < 2; ++u) {
    const int c = tid + 512 * u;
    asrc[u] = A + (size_t)(row0 + (c >> 3)) * lda + (c & 7) * 8;
    adst[u] = (c >> 3) * GA_PITCH + (c & 7) * 16;
    const int kr = c >> 4, ch = c & 15;
    if (MODE == 2) {
      const int blk = ch >> 2;
      bsrc[u] = ((blk & 1) ? B1 : B0) + (size_t)kr * ldb + col0 + (blk >> 1) * 32 + (ch & 3) * 8;
    } else {
      bsrc[u] = B0 + (size_t)kr * ldb + col0 + ch * 8;
    }
    bdst[u] = off_b(kr, ch);
  }
  u32x4 ra[2], rb[2];
#pragma unroll
  for (int u = 0; u < 2; ++u) { ra[u] = *(const u32x4*)asrc[u]; rb[u] = *(const u32x4*)bsrc[u]; }
  const int nkt = K >> 6;
  for (int kt = 0; kt < nkt; ++kt) {
#pragma unroll
    for (int u = 0; u < 2; ++u) { *(u32x4*)(la + adst[u]) = ra[u]; *(u32x4*)(lb + bdst[u]) = rb[u]; }
    __syncthreads();
    if (kt + 1 < nkt) {
#pragma unroll
      for (int u = 0; u < 2; ++u) {
        ra[u] = *(const u32x4*)(asrc[u] + (kt + 1) * 64);
        rb[u] = *(const u32x4*)(bsrc[u] + (size_t)(kt + 1) * 64 * ldb);
      }
    }
#pragma unroll
    for (int ks = 0; ks < 2; ++ks) {
      bf16x8 af[2], bfr_[4];
#pragma unroll
      for (int tt = 0; tt < 2; ++tt) af[tt] = *(const bf16x8*)(la + (wm * 32 + tt * 16 + li) * GA_PITCH + ks * 64 + g * 16);
#pragma unroll
      for (int nt = 0; nt < 4; ++nt) {
        const unsigned ch = 2 * (wn * 4 + nt) + (p >> 1);
        bf16x4 lo = tr_read(lb, off_b(32 * ks + 8 * g + q, ch) + 8 * (p & 1));
        bf16x4 hi = tr_read(lb, off_b(32 * ks + 8 * g + 4 + q, ch) + 8 * (p & 1));
        bfr_[nt] = cat(lo, hi);
      }
#pragma unroll
      for (int tt = 0; tt < 2; ++tt)
#pragma unroll
        for (int nt = 0; nt < 4; ++nt) {
          if (MODE == 1) acc[tt][nt] = mfma32(af[tt], bfr_[nt], acc[tt][nt]);
          else acc[tt][nt] = mfma32(bfr_[nt], af[tt], acc[tt][nt]);
        }
    }
    __syncthreads();
  }
  if (MODE == 0) {
    const int seg = (row0 >> 5) + wm, head = col0 >> 7;
#pragma unroll
    for (int tt = 0; tt < 2; ++tt)
#pragma unroll
      for (int k2 = 0; k2 < 2; ++k2)
        st16(rs, dst + ((((seg * NH + head) * 2 + tt) * 4 + (wn * 2 + k2)) * 64 + lane) * 16, cat(pk4(acc[tt][2 * k2]), pk4(acc[tt][2 * k2 + 1])));
  } else if (MODE == 1) {
    const int seg = (row0 >> 5) + wm, head = col0 >> 7;
#pragma unroll
    for (int nt = 0; nt < 4; ++nt)
      st16(rs, dst + (((seg * NH + head) * 8 + (wn * 4 + nt)) * 64 + lane) * 16, cat(pk4(acc[0][nt]), pk4(acc[1][nt])));
  } else if (MODE == 2) {
#pragma unroll
    for (int tt = 0; tt < 2; ++tt)
#pragma unroll
      for (int n2 = 0; n2 < 2; ++n2) {
        f32x4 hv;
#pragma unroll
        for (int r = 0; r < 4; ++r) {
          const float gg = bfr(acc[tt][n2][r]), uu = bfr(acc[tt][n2 + 2][r]);
          const float sg = bfr(1.f / (1.f + __expf(-gg)));
          hv[r] = sg * uu;
        }
        const int row = row0 + wm * 32 + tt * 16 + li, col = col0 + 32 * wn + 16 * n2 + 4 * g;
        st8(rs, dst + (row * FF + col) * 2, pk4(hv));
      }
  } else {
#pragma unroll
    for (int tt = 0; tt < 2; ++tt)
#pragma unroll
      for (int nt = 0; nt < 4; ++nt) {
        const int row = row0 + wm * 32 + tt * 16 + li, col = col0 + 64 * wn + 16 * nt + 4 * g;
        st8(rs, dst + (row * DM + col) * 2, pk4(acc[tt][nt]));
      }
  }
}

__device__ __forceinline__ void xattn_unit(char* ws, rsrc_t rs, int seg, int head) {
  const int lane = threadIdx.x & 63, li = lane & 15, g = lane >> 4;
  const int unit = seg * NH + head;
  const bf16* qf = (const bf16*)(ws + OFF_QF) + (size_t)unit * 4096;
  const bf16* kf = (const bf16*)(ws + OFF_KF) + (size_t)unit * 4096;
  const bf16* vf = (const bf16*)(ws + OFF_VF) + (size_t)unit * 4096;
  const bf16* km = (const bf16*)(ws + OFF_KMH) + (size_t)unit * 2048;
  const bf16* vm = (const bf16*)(ws + OFF_VMH) + (size_t)unit * 2048;
  const float scale = 0.08838834764831845f;
  f32x4 sc[3][2];
#pragma unroll
  for (int a = 0; a < 3; ++a) { sc[a][0] = (f32x4){0, 0, 0, 0}; sc[a][1] = (f32x4){0, 0, 0, 0}; }
#pragma unroll
  for (int kk = 0; kk < 4; ++kk) {
    const bf16x8 q0 = *(const bf16x8*)(qf + (0 * 4 + kk) * 512 + lane * 8), q1 = *(const bf16x8*)(qf + (1 * 4 + kk) * 512 + lane * 8);
    const bf16x8 k0 = *(const bf16x8*)(km + kk * 512 + lane * 8);
    const bf16x8 k1 = *(const bf16x8*)(kf + (0 * 4 + kk) * 512 + lane * 8), k2 = *(const bf16x8*)(kf + (1 * 4 + kk) * 512 + lane * 8);
    sc[0][0] = mfma32(k0, q0, sc[0][0]); sc[0][1] = mfma32(k0, q1, sc[0][1]);
    sc[1][0] = mfma32(k1, q0, sc[1][0]); sc[1][1] = mfma32(k1, q1, sc[1][1]);
    sc[2][0] = mfma32(k2, q0, sc[2][0]); sc[2][1] = mfma32(k2, q1, sc[2][1]);
  }
  bf16x4 p0[2]; bf16x8 p12[2];
#pragma unroll
  for (int qt = 0; qt < 2; ++qt) {
    const int qi = 16 * qt + li;
    float v[12]; float m = -3.0e38f;
#pragma unroll
    for (int kt = 0; kt < 3; ++kt)
#pragma unroll
      for (int r = 0; r < 4; ++r) {
        float s = bfr(bfr(sc[kt][qt][r]) * scale);
        bool ok = (kt == 0) ? (g < 2) : (16 * (kt - 1) + 4 * g + r <= qi);
        s = ok ? s : -3.0e38f;
        v[kt * 4 + r] = s; m = fmaxf(m, s);
      }
    m = xmax16_32(m);
    float sum = 0.f;
#pragma unroll
    for (int e = 0; e < 12; ++e) { v[e] = (v[e] > -1.0e38f) ? __expf(v[e] - m) : 0.f; sum += v[e]; }
    sum += __shfl_xor(sum, 16); sum += __shfl_xor(sum, 32);
    const float inv = 1.f / sum;
#pragma unroll
    for (int r = 0; r < 4; ++r) { p0[qt][r] = (__bf16)(v[r] * inv); p12[qt][r] = (__bf16)(v[4 + r] * inv); p12[qt][4 + r] = (__bf16)(v[8 + r] * inv); }
  }
#pragma unroll
  for (int kk = 0; kk < 4; ++kk) {
    f32x4 o[2][2];
#pragma unroll
    for (int e2 = 0; e2 < 2; ++e2) {
      const int et = 2 * kk + e2;
      const bf16x4 vmf = *(const bf16x4*)(vm + et * 256 + lane * 4);
      const bf16x8 vxf = *(const bf16x8*)(vf + et * 512 + lane * 8);
#pragma unroll
      for (int qt = 0; qt < 2; ++qt) {
        o[qt][e2] = mfma16(vmf, p0[qt], (f32x4){0, 0, 0, 0});
        o[qt][e2] = mfma32(vxf, p12[qt], o[qt][e2]);
      }
    }
#pragma unroll
    for (int qt = 0; qt < 2; ++qt)
      st16(rs, (unsigned)OFF_OX + ((((seg * 2 + qt) * NH + head) * 4 + kk) * 64 + lane) * 16, cat(pk4(o[qt][0]), pk4(o[qt][1])));
  }
}

__device__ __forceinline__ void wof_task(const bf16* __restrict__ Wo, char* lds, rsrc_t rs, int t) {
  const int tid = threadIdx.x, lane = tid & 63, w = tid >> 6, li = lane & 15, g = lane >> 4, q4 = li >> 2, p4 = li & 3;
  const int h = t >> 1, chh = t & 1;
  constexpr int P = 528;
#pragma unroll
  for (int u = 0; u < 8; ++u) {
    const int cidx = tid + 512 * u, rr = cidx >> 5, ch = cidx & 31;
    *(u32x4*)(lds + rr * P + ch * 16) = *(const u32x4*)(Wo + ((size_t)h * DH + rr) * DM + chh * 256 + ch * 8);
  }
  __syncthreads();
#pragma unroll
  for (int n2 = 0; n2 < 2; ++n2)
#pragma unroll
    for (int kk = 0; kk < 4; ++kk) {
      const int ntl = 2 * w + n2;
      bf16x4 lo = tr_read(lds, ((2 * kk + 0) * 16 + 4 * g + q4) * P + (16 * ntl + 4 * p4) * 2);
      bf16x4 hi = tr_read(lds, ((2 * kk + 1) * 16 + 4 * g + q4) * P + (16 * ntl + 4 * p4) * 2);
      st16(rs, (unsigned)OFF_WOF + ((((chh * 16 + ntl) * NH + h) * 4 + kk) * 64 + lane) * 16, cat(lo, hi));
    }
}

__device__ __forceinline__ void final_task(char* ws, char* lds, const bf16* __restrict__ x, bf16* __restrict__ out, int seg, int cb) {
  const int tid = threadIdx.x, lane = tid & 63, w = tid >> 6;
  const bf16* oxf = (const bf16*)(ws + OFF_OX);
  const bf16* wof = (const bf16*)(ws + OFF_WOF);
  f32x4 acc[2][4];
#pragma unroll
  for (int a = 0; a < 2; ++a)
#pragma unroll
    for (int b = 0; b < 4; ++b) acc[a][b] = (f32x4){0.f, 0.f, 0.f, 0.f};
#pragma unroll 4
  for (int i = 0; i < 16; ++i) {
    const int hk = w * 16 + i;
    bf16x8 af[2], bf[4];
#pragma unroll
    for (int tt = 0; tt < 2; ++tt) af[tt] = *(const bf16x8*)(oxf + ((size_t)(seg * 2 + tt) * 128 + hk) * 512 + lane * 8);
#pragma unroll
    for (int nt = 0; nt < 4; ++nt) bf[nt] = *(const bf16x8*)(wof + ((size_t)(cb * 4 + nt) * 128 + hk) * 512 + lane * 8);
#pragma unroll
    for (int tt = 0; tt < 2; ++tt)
#pragma unroll
      for (int nt = 0; nt < 4; ++nt) acc[tt][nt] = mfma32(bf[nt], af[tt], acc[tt][nt]);
  }
#pragma unroll
  for (int tt = 0; tt < 2; ++tt)
#pragma unroll
    for (int nt = 0; nt < 4; ++nt) *(f32x4*)(lds + (((w * 2 + tt) * 4 + nt) * 64 + lane) * 16) = acc[tt][nt];
  __syncthreads();
  {
    const int tt = tid >> 8, nt = (tid >> 6) & 3, li = lane & 15, g = lane >> 4;
    f32x4 a = {0, 0, 0, 0};
#pragma unroll
    for (int ww = 0; ww < 8; ++ww) a += *(const f32x4*)(lds + (((ww * 2 + tt) * 4 + nt) * 64 + lane) * 16);
    const int row = seg * 32 + tt * 16 + li, col = cb * 64 + nt * 16 + 4 * g;
    const bf16x4 xv = *(const bf16x4*)(x + (size_t)row * DM + col);
    const bf16x4 bv = *(const bf16x4*)((const bf16*)(ws + OFF_BOUT) + (size_t)row * DM + col);
    f32x4 o;
#pragma unroll
    for (int r = 0; r < 4; ++r) o[r] = bfr((float)xv[r] + bfr(a[r])) + (float)bv[r];
    *(bf16x4*)(out + (size_t)row * DM + col) = pk4(o);
  }
}

__device__ __forceinline__ unsigned* cnt_ptr(char* ws, int idx) { return (unsigned*)(ws + OFF_CNT) + idx * 64; }
__device__ __forceinline__ void wait_cnt(char* ws, int idx, unsigned target) {
  unsigned spins = 0;
  while (__hip_atomic_load(cnt_ptr(ws, idx), RLX_AGENT) < target) { __builtin_amdgcn_s_sleep(8); if (++spins > (1u << 22)) break; }
}
__device__ __forceinline__ void acquire_all() {
  if (threadIdx.x == 0) { __builtin_amdgcn_fence(__ATOMIC_ACQUIRE, "agent"); asm volatile("s_waitcnt vmcnt(0)" ::: "memory"); }
  __syncthreads();
}
__device__ __forceinline__ void signal_cnt(char* ws, int idx) {
  asm volatile("s_waitcnt vmcnt(0)" ::: "memory");
  __syncthreads();
  if (threadIdx.x == 0) __hip_atomic_fetch_add(cnt_ptr(ws, idx), 1u, RLX_AGENT);
}

constexpr int T_KV = 0, T_Q = T_KV + 1024, T_G = T_Q + 512, T_W = T_G + 448, T_B = T_W + 64, T_S = T_B + 64, T_END = T_S + 64 * 12;

__device__ __forceinline__ void worker(const bf16* __restrict__ x, const bf16* __restrict__ Wq, const bf16* __restrict__ Wk, const bf16* __restrict__ Wv,
                                       const bf16* __restrict__ Wo, const bf16* __restrict__ Wg, const bf16* __restrict__ Wu, const bf16* __restrict__ Wd,
                                       bf16* __restrict__ out, char* ws, char* lds) {
  const rsrc_t rs = __builtin_amdgcn_make_buffer_rsrc(ws, 0, WS_SPAN, 0x00020000);
  const bf16* xn = (const bf16*)(ws + OFF_XN);
  volatile int* tk = (volatile int*)(lds + 144 * 1024);
  for (;;) {
    __syncthreads();
    if (threadIdx.x == 0) *tk = (int)__hip_atomic_fetch_add(cnt_ptr(ws, C_TICKET), 1u, RLX_AGENT);
    __syncthreads();
    const int t = *tk;
    if (t >= T_END) break;
    if (t < T_Q) {
      const int rb = t >> 6, cb = t & 63;
      if (cb < 32) gemm_tile<0>(xn, DM, Wk, nullptr, NH * DH, DM, rb * 128, cb * 128, lds, rs, (unsigned)OFF_KF);
      else gemm_tile<1>(xn, DM, Wv, nullptr, NH * DH, DM, rb * 128, (cb - 32) * 128, lds, rs, (unsigned)OFF_VF);
      signal_cnt(ws, C_KV + rb);
    } else if (t < T_G) {
      const int u = t - T_Q, rb = u >> 5, cb = u & 31;
      gemm_tile<0>(xn, DM, Wq, nullptr, NH * DH, DM, rb * 128, cb * 128, lds, rs, (unsigned)OFF_QF);
      signal_cnt(ws, C_Q + rb);
    } else if (t < T_W) {
      const int u = t - T_G, rb = u / 28, cb = u - rb * 28;
      gemm_tile<2>(xn, DM, Wg, Wu, FF, DM, rb * 128, cb * 64, lds, rs, (unsigned)OFF_H);
      signal_cnt(ws, C_H + rb);
    } else if (t < T_B) {
      wof_task(Wo, lds, rs, t - T_W);
      signal_cnt(ws, C_WOF);
    } else if (t < T_S) {
      const int u = t - T_B, rb = u >> 2, cb = u & 3;
      if (threadIdx.x == 0) wait_cnt(ws, C_H + rb, 28);
      acquire_all();
      gemm_tile<3>((const bf16*)(ws + OFF_H), FF, Wd, nullptr, DM, FF, rb * 128, cb * 128, lds, rs, (unsigned)OFF_BOUT);
      signal_cnt(ws, C_BO + rb);
    } else {
      const int u = t - T_S, seg = u / 12, k = u - seg * 12, rb = seg >> 2;
      if (k < 4) {
        if (threadIdx.x == 0) { wait_cnt(ws, C_Q + rb, 32); wait_cnt(ws, C_KV + rb, 64); wait_cnt(ws, C_CH + seg, 256); }
        acquire_all();
        xattn_unit(ws, rs, seg, k * 8 + (threadIdx.x >> 6));
        signal_cnt(ws, C_X + seg);
      } else {
        if (threadIdx.x == 0) { wait_cnt(ws, C_WOF, 64); wait_cnt(ws, C_BO + rb, 4); wait_cnt(ws, C_X + seg, 4); }
        acquire_all();
        final_task(ws, lds, x, out, seg, k - 4);
      }
    }
  }
}

constexpr int CL_SCR = 0;
constexpr int CL_KX = 0;
constexpr int CL_VX = CL_KX + 8192;
constexpr int CL_MEM = CL_VX + 4096;
constexpr int CL_QF = CL_MEM + 8192;
constexpr int CL_KF = CL_QF + 4096;
constexpr int CL_OF = CL_KF + 4096;
constexpr int CL_P = CL_OF + 2048;
constexpr int CL_WV = 32768;
constexpr int CL_WO = 98304;
constexpr int CHAIN_LDS = 163840;
#define STAMP(k)
constexpr unsigned SENT = 0xFFFFFFFFu;
constexpr unsigned SPIN_CAP = 1u << 22;

__device__ __forceinline__ void chain(const bf16* __restrict__ Wq, const bf16* __restrict__ Wk, const bf16* __restrict__ Wv,
                                      const bf16* __restrict__ Wo, char* ws, char* lds) {
  const int tid = threadIdx.x, lane = tid & 63, w = __builtin_amdgcn_readfirstlane(tid >> 6);
  const int li = lane & 15, g = lane >> 4, q4 = li >> 2, p4 = li & 3;
  const int b = blockIdx.x, h = b >> 1, c = b & 1;
  bf16x8 wqk[2][16];
  {
    constexpr int P1 = 272;
    char* scr = lds + 32768;
#pragma unroll
    for (int it = 0; it < 4; ++it) {
#pragma unroll
      for (int u = 0; u < 8; ++u) {
        const int cidx = tid + 512 * u;
        const int mat = cidx >> 11, rr = (cidx >> 4) & 127, ch = cidx & 15;
        const bf16* src = (mat ? Wk : Wq) + (size_t)(128 * it + rr) * (NH * DH) + h * DH + ch * 8;
        *(u32x4*)(scr + mat * (128 * P1) + rr * P1 + ch * 16) = *(const u32x4*)src;
      }
      __syncthreads();
      const char* base = scr + (w >> 2) * (128 * P1);
#pragma unroll
      for (int k4 = 0; k4 < 4; ++k4)
#pragma unroll
        for (int t = 0; t < 2; ++t) {
          const int c16 = 2 * (w & 3) + t;
          bf16x4 lo = tr_read(base, (32 * k4 + 8 * g + q4) * P1 + (16 * c16 + 4 * p4) * 2);
          bf16x4 hi = tr_read(base, (32 * k4 + 8 * g + 4 + q4) * P1 + (16 * c16 + 4 * p4) * 2);
          wqk[t][4 * it + k4] = cat(lo, hi);
        }
      __syncthreads();
    }
    constexpr int P2 = 1040;
    scr = lds;
#pragma unroll
    for (int u = 0; u < 8; ++u) {
      const int cidx = tid + 512 * u;
      const int rr = cidx >> 6, ch = cidx & 63;
      *(u32x4*)(scr + rr * P2 + ch * 16) = *(const u32x4*)(Wo + ((size_t)h * DH + c * 64 + rr) * DM + ch * 8);
    }
    __syncthreads();
#pragma unroll
    for (int nt = 0; nt < 4; ++nt)
#pragma unroll
      for (int k2 = 0; k2 < 2; ++k2) {
        const int col = 64 * w + 16 * nt + 4 * p4;
        bf16x4 lo = tr_read(scr, ((2 * k2 + 0) * 16 + 4 * g + q4) * P2 + col * 2);
        bf16x4 hi = tr_read(scr, ((2 * k2 + 1) * 16 + 4 * g + q4) * P2 + col * 2);
        *(bf16x8*)(lds + CL_WO + (((w * 4 + nt) * 2 + k2) * 64 + lane) * 16) = cat(lo, hi);
      }
    __syncthreads();
    constexpr int P3 = 144;
#pragma unroll
    for (int it = 0; it < 4; ++it) {
#pragma unroll
      for (int u = 0; u < 2; ++u) {
        const int cidx = tid + 512 * u;
        const int rr = cidx >> 3, ch = cidx & 7;
        *(u32x4*)(scr + rr * P3 + ch * 16) = *(const u32x4*)(Wv + (size_t)(128 * it + rr) * (NH * DH) + h * DH + c * 64 + ch * 8);
      }
      __syncthreads();
#pragma unroll
      for (int e2 = 0; e2 < 2; ++e2) {
        const int et = 2 * (w & 1) + e2, k4 = w >> 1;
        bf16x4 lo = tr_read(scr, (32 * k4 + 8 * g + q4) * P3 + (16 * et + 4 * p4) * 2);
        bf16x4 hi = tr_read(scr, (32 * k4 + 8 * g + 4 + q4) * P3 + (16 * et + 4 * p4) * 2);
        *(bf16x8*)(lds + CL_WV + ((et * 16 + 4 * it + k4) * 64 + lane) * 16) = cat(lo, hi);
      }
      __syncthreads();
    }
  }
  const auto rs_mem = __builtin_amdgcn_make_buffer_rsrc(ws + OFF_MEMH, 0, 65 * 8192, 0x00020000);
  const auto rs_part = __builtin_amdgcn_make_buffer_rsrc(ws + OFF_PART, 0, 1 << 20, 0x00020000);
  const auto rs_km = __builtin_amdgcn_make_buffer_rsrc(ws + OFF_KMH, 0, 16 << 20, 0x00020000);
  const float scale = 0.08838834764831845f;
  const auto rs_kv = __builtin_amdgcn_make_buffer_rsrc(ws + OFF_KF, 0, 32 << 20, 0x00020000);
  unsigned rdy = 0;
  if (w < 4) {
    unsigned spins = 0;
    while (__builtin_amdgcn_readfirstlane(__hip_atomic_load(cnt_ptr(ws, C_KV), RLX_AGENT)) < 64u) { __builtin_amdgcn_s_sleep(4); if (++spins > SPIN_CAP) break; }
    const u32x4 a0 = __builtin_amdgcn_raw_buffer_load_b128(rs_kv, lane * 16, h * 8192 + w * 2048, 16);
    const u32x4 a1 = __builtin_amdgcn_raw_buffer_load_b128(rs_kv, lane * 16, h * 8192 + w * 2048 + 1024, 16);
    const u32x4 a2 = __builtin_amdgcn_raw_buffer_load_b128(rs_kv, lane * 16, (16 << 20) + h * 8192 + c * 4096 + w * 1024, 16);
    *(u32x4*)(lds + lane * 16 + CL_KX + w * 2048) = a0;
    *(u32x4*)(lds + lane * 16 + CL_KX + w * 2048 + 1024) = a1;
    *(u32x4*)(lds + lane * 16 + CL_VX + w * 1024) = a2;
    rdy = 64u;
  }
  const int pst_off = (((8 * w + 2 * (g & 1) + (g >> 1)) * 64 + b) * 8 + li) * 16;
  const char* l16 = lds + lane * 16;
  const char* mfb = lds + CL_MEM + (g * 8 + (li & 7)) * 16;
  const char* wvb = lds + CL_WV + ((w & 3) * 1024 + lane) * 16;
  const char* wob = lds + CL_WO + (w * 512 + lane) * 16;
  for (int s = 0; s < NSEG; ++s) {
    if (w >= 6) {
      u32x4 v[4]; unsigned spins = 0;
      asm volatile("s_waitcnt vmcnt(0)" ::: "memory");
      for (;;) {
        asm volatile("" ::: "memory");
#pragma unroll
        for (int u = 0; u < 4; ++u) v[u] = __builtin_amdgcn_raw_buffer_load_b128(rs_mem, lane * 16, s * 8192 + (w - 6) * 4096 + u * 1024, 16);
        unsigned m = 0;
#pragma unroll
        for (int u = 0; u < 4; ++u) m = max(m, max(v[u][0], v[u][2]));
        if (__all(m != SENT)) break;
        if (++spins > SPIN_CAP) break;
      }
#pragma unroll
      for (int u = 0; u < 4; ++u) *(u32x4*)(l16 + CL_MEM + (w - 6) * 4096 + u * 1024) = v[u];
    }
    STAMP(0);
    __syncthreads();
    STAMP(1);
    u32x4 pf[3];
    const int sn = (s + 1 < NSEG) ? s + 1 : s;
    if (w < 4) {
      if (__builtin_amdgcn_readfirstlane(rdy) < 64u) {
        unsigned spins = 0;
        while (__builtin_amdgcn_readfirstlane(__hip_atomic_load(cnt_ptr(ws, C_KV + (sn >> 2)), RLX_AGENT)) < 64u) { __builtin_amdgcn_s_sleep(2); if (++spins > SPIN_CAP) break; }
      }
      const int so = (sn * NH + h) * 8192;
      pf[0] = __builtin_amdgcn_raw_buffer_load_b128(rs_kv, lane * 16, so + w * 2048, 16);
      pf[1] = __builtin_amdgcn_raw_buffer_load_b128(rs_kv, lane * 16, so + w * 2048 + 1024, 16);
      pf[2] = __builtin_amdgcn_raw_buffer_load_b128(rs_kv, lane * 16, (16 << 20) + so + c * 4096 + w * 1024, 16);
      const int s2 = (s + 2 < NSEG) ? s + 2 : NSEG - 1;
      rdy = __hip_atomic_load(cnt_ptr(ws, C_KV + (s2 >> 2)), RLX_AGENT);
    }
    bf16x8 mf[16];
#pragma unroll
    for (int ks = 0; ks < 16; ++ks) mf[ks] = *(const bf16x8*)(mfb + ks * 512);
    {
      f32x4 a0 = {0, 0, 0, 0}, a1 = {0, 0, 0, 0};
#pragma unroll
      for (int ks = 0; ks < 16; ++ks) {
        a0 = mfma32(wqk[0][ks], mf[ks], a0);
        a1 = mfma32(wqk[1][ks], mf[ks], a1);
      }
      const bf16x8 o = cat(pk4(a0), pk4(a1));
      *(bf16x8*)(lds + (w < 4 ? CL_QF : CL_KF) + ((w & 3) * 64 + lane) * 16) = o;
      if (w >= 4 && c == 0) __builtin_amdgcn_raw_buffer_store_b128(__builtin_bit_cast(u32x4, o), rs_km, lane * 16, ((s * NH + h) * 4 + (w & 3)) * 1024, 16);
    }
    STAMP(2);
    bf16x4 vm4 = {(__bf16)0.f, (__bf16)0.f, (__bf16)0.f, (__bf16)0.f};
    if (w == 0) {
      bf16x8 kxf[8];
#pragma unroll
      for (int u = 0; u < 8; ++u) kxf[u] = *(const bf16x8*)(l16 + CL_KX + u * 1024);
      __syncthreads();
      f32x4 s0 = {0, 0, 0, 0}, s1 = {0, 0, 0, 0}, s2 = {0, 0, 0, 0};
#pragma unroll
      for (int kk = 0; kk < 4; ++kk) {
        const bf16x8 qf = *(const bf16x8*)(l16 + CL_QF + kk * 1024);
        const bf16x8 k0 = *(const bf16x8*)(l16 + CL_KF + kk * 1024);
        s0 = mfma32(k0, qf, s0); s1 = mfma32(kxf[kk], qf, s1); s2 = mfma32(kxf[4 + kk], qf, s2);
      }
      if (g >= 2) s0 = (f32x4){-3.0e38f, -3.0e38f, -3.0e38f, -3.0e38f};
      float m = fmaxf(fmaxf(fmaxf(s0[0], s0[1]), fmaxf(s0[2], s0[3])), fmaxf(fmaxf(fmaxf(s1[0], s1[1]), fmaxf(s1[2], s1[3])), fmaxf(fmaxf(s2[0], s2[1]), fmaxf(s2[2], s2[3]))));
      m = xmax16_32(m);
      const float cexp = scale * 1.4426950408889634f, mc = -m * cexp;
      bf16x4 p0; bf16x8 p12;
#pragma unroll
      for (int r = 0; r < 4; ++r) {
        p0[r] = (__bf16)__builtin_amdgcn_exp2f(fmaf(s0[r], cexp, mc));
        p12[r] = (__bf16)__builtin_amdgcn_exp2f(fmaf(s1[r], cexp, mc));
        p12[4 + r] = (__bf16)__builtin_amdgcn_exp2f(fmaf(s2[r], cexp, mc));
      }
      *(bf16x8*)(lds + CL_P + lane * 16) = p12;
      *(bf16x4*)(lds + CL_P + 1024 + lane * 8) = p0;
    } else {
      if (w >= 4) {
        f32x4 va = {0, 0, 0, 0}, vb = {0, 0, 0, 0};
        bf16x8 wv[2][4];
#pragma unroll
        for (int k1 = 0; k1 < 4; ++k1) wv[0][k1] = *(const bf16x8*)(wvb + k1 * 1024);
        __syncthreads();
#pragma unroll
        for (int k4 = 0; k4 < 4; ++k4) {
          if (k4 < 3) {
#pragma unroll
            for (int k1 = 0; k1 < 4; ++k1) wv[(k4 + 1) & 1][k1] = *(const bf16x8*)(wvb + (4 * k4 + 4 + k1) * 1024);
          }
#pragma unroll
          for (int k1 = 0; k1 < 4; k1 += 2) { va = mfma32(mf[4 * k4 + k1], wv[k4 & 1][k1], va); vb = mfma32(mf[4 * k4 + k1 + 1], wv[k4 & 1][k1 + 1], vb); }
          __builtin_amdgcn_sched_barrier(0);
        }
        vm4 = pk4(va + vb);
        __builtin_amdgcn_raw_buffer_store_b64(__builtin_bit_cast(u32x2, vm4), rs_km, lane * 8, (8 << 20) + ((s * NH + h) * 8 + 4 * c + (w - 4)) * 512, 16);
      } else {
        __syncthreads();
      }
    }
    __syncthreads();
    if (w >= 4) {
      const bf16x8 p12 = *(const bf16x8*)(lds + CL_P + lane * 16);
      const bf16x4 p0 = *(const bf16x4*)(lds + CL_P + 1024 + lane * 8);
      const bf16x8 vx8 = *(const bf16x8*)(lds + CL_VX + ((w - 4) * 64 + lane) * 16);
      f32x4 o = mfma16(vm4, p0, (f32x4){0, 0, 0, 0});
      const bf16x4 one4 = {(__bf16)1.f, (__bf16)1.f, (__bf16)1.f, (__bf16)1.f};
      f32x4 l = mfma16(one4, p0, (f32x4){0, 0, 0, 0});
      o = mfma32(vx8, p12, o);
      l = mfma32(cat(one4, one4), p12, l);
      const float inv = __builtin_amdgcn_rcpf(l[0]);
      *(bf16x4*)(lds + CL_OF + (((w - 4) >> 1) * 64 + lane) * 16 + (w & 1) * 8) = pk4(o * inv);
    }
    bf16x8 wo[4][2];
#pragma unroll
    for (int nt = 0; nt < 4; ++nt)
#pragma unroll
      for (int k2 = 0; k2 < 2; ++k2) wo[nt][k2] = *(const bf16x8*)(wob + (nt * 2 + k2) * 1024);
    STAMP(3);
    __syncthreads();
    STAMP(4);
    if (s + 1 == NSEG) {
      if (w >= 4) { asm volatile("s_waitcnt vmcnt(0)" ::: "memory"); if (lane == 0) __hip_atomic_fetch_add(cnt_ptr(ws, C_CH + s), 1u, RLX_AGENT); }
      break;
    }
    const int slot = (s & 1) << 19;
    {
      const bf16x8 of0 = *(const bf16x8*)(l16 + CL_OF);
      const bf16x8 of1 = *(const bf16x8*)(l16 + CL_OF + 1024);
#pragma unroll
      for (int n2 = 0; n2 < 2; ++n2) {
        f32x4 pa = mfma32(wo[2 * n2][0], of0, (f32x4){0, 0, 0, 0});
        f32x4 pb = mfma32(wo[2 * n2 + 1][0], of0, (f32x4){0, 0, 0, 0});
        pa = mfma32(wo[2 * n2][1], of1, pa);
        pb = mfma32(wo[2 * n2 + 1][1], of1, pb);
        u32x2 A = __builtin_bit_cast(u32x2, pk4(pa)), B = __builtin_bit_cast(u32x2, pk4(pb));
        auto r0 = __builtin_amdgcn_permlane16_swap(A[0], B[0], false, false);
        auto r1 = __builtin_amdgcn_permlane16_swap(A[1], B[1], false, false);
        u32x4 pv = {r0[0] | 1u, r1[0], r0[1] | 1u, r1[1]};
        if (li < 8) __builtin_amdgcn_raw_buffer_store_b128(pv, rs_part, pst_off, slot + n2 * 32768, 16);
      }
    }
    STAMP(5);
    if (w < 4) {
      *(u32x4*)(l16 + CL_KX + w * 2048) = pf[0];
      *(u32x4*)(l16 + CL_KX + w * 2048 + 1024) = pf[1];
      *(u32x4*)(l16 + CL_VX + w * 1024) = pf[2];
    }
    if (w == 4 || w == 5) { asm volatile("s_waitcnt vmcnt(0)" ::: "memory"); if (lane == 0) __hip_atomic_fetch_add(cnt_ptr(ws, C_CH + s), 1u, RLX_AGENT); }
    if (w >= 6) {
      u32x2 v[8]; unsigned spins = 0;
      const int roff = lane * 16 + (w - 6) * 8;
      asm volatile("s_waitcnt vmcnt(0)" ::: "memory");
      for (;;) {
        asm volatile("" ::: "memory");
#pragma unroll
        for (int it = 0; it < 8; ++it) v[it] = __builtin_amdgcn_raw_buffer_load_b64(rs_part, roff, slot + b * 8192 + it * 1024, 16);
        unsigned m = 1u;
#pragma unroll
        for (int it = 0; it < 8; ++it) m &= v[it][0];
        if (__all(m != 0u)) break;
        if (++spins > SPIN_CAP) break;
      }
      STAMP(6);
      f32x4 a = {0, 0, 0, 0};
#pragma unroll
      for (int it = 0; it < 8; ++it) {
        a[0] += __builtin_bit_cast(float, v[it][0] << 16); a[1] += __builtin_bit_cast(float, v[it][0] & 0xffff0000u);
        a[2] += __builtin_bit_cast(float, v[it][1] << 16); a[3] += __builtin_bit_cast(float, v[it][1] & 0xffff0000u);
      }
#pragma unroll
      for (int r = 0; r < 4; ++r) a[r] = xsum8_16_32(a[r]);
      u32x2 o = __builtin_bit_cast(u32x2, pk4(a));
      if (o[0] == SENT) o[0] = 0x7FFF7FFFu;
      if (lane < 8) __builtin_amdgcn_raw_buffer_store_b64(o, rs_mem, lane * 16 + (w - 6) * 8, (s + 1) * 8192 + b * 128, 16);
      if (lane == 0) __hip_atomic_fetch_add(cnt_ptr(ws, C_CH + s), 1u, RLX_AGENT);
      const u32x2 z = {0u, 0u};
#pragma unroll
      for (int it = 0; it < 8; ++it) __builtin_amdgcn_raw_buffer_store_b64(z, rs_part, roff, slot + b * 8192 + it * 1024, 16);
      STAMP(7);
    }
  }
}

__global__ __launch_bounds__(512, 2) void k_main(const bf16* __restrict__ x, const bf16* __restrict__ Wq, const bf16* __restrict__ Wk,
                                                 const bf16* __restrict__ Wv, const bf16* __restrict__ Wo, const bf16* __restrict__ Wg,
                                                 const bf16* __restrict__ Wu, const bf16* __restrict__ Wd, bf16* __restrict__ out, char* ws) {
  extern __shared__ __attribute__((aligned(16))) char lds[];
  if (blockIdx.x < 64) chain(Wq, Wk, Wv, Wo, ws, lds);
  else worker(x, Wq, Wk, Wv, Wo, Wg, Wu, Wd, out, ws, lds);
}

extern "C" void kernel_launch(void* const* d_in, const int* in_sizes, int n_in,
                              void* d_out, int out_size, void* d_ws, size_t ws_size,
                              hipStream_t stream) {
  const bf16* x = (const bf16*)d_in[0];
  const bf16* mem0 = (const bf16*)d_in[1];
  const bf16* Wq = (const bf16*)d_in[2];
  const bf16* Wk = (const bf16*)d_in[3];
  const bf16* Wv = (const bf16*)d_in[4];
  const bf16* Wo = (const bf16*)d_in[5];
  const bf16* Wg = (const bf16*)d_in[8];
  const bf16* Wu = (const bf16*)d_in[9];
  const bf16* Wd = (const bf16*)d_in[10];
  char* ws = (char*)d_ws;
  (void)hipFuncSetAttribute((const void*)k_main, hipFuncAttributeMaxDynamicSharedMemorySize, CHAIN_LDS);
  (void)hipMemsetAsync(ws + OFF_MEMH, 0xFF, 65 * 8192, stream);
  (void)hipMemsetAsync(ws + OFF_PART, 0, (1 << 20) + 65536, stream);
  k_ln<<<S_ / 4, 256, 0, stream>>>(x, mem0, ws);
  k_main<<<256, 512, CHAIN_LDS, stream>>>(x, Wq, Wk, Wv, Wo, Wg, Wu, Wd, (bf16*)d_out, ws);
}
```

```cpp
#include <hip/hip_runtime.h>
#include <hip/hip_bf16.h>

constexpr int S_ = 2048, DM = 512, NH = 32, DH = 128, MM = 8, SEG = 32, NSEG = 64, FF = 1792;

typedef __bf16 bf16;
typedef __bf16 bf16x8 __attribute__((ext_vector_type(8)));
typedef __bf16 bf16x4 __attribute__((ext_vector_type(4)));
typedef short s16x4 __attribute__((ext_vector_type(4)));
typedef float f32x4 __attribute__((ext_vector_type(4)));
typedef unsigned u32x4 __attribute__((ext_vector_type(4)));
typedef unsigned u32x2 __attribute__((ext_vector_type(2)));
#define LAS __attribute__((address_space(3)))

constexpr size_t OFF_MEMH = 0;
constexpr size_t OFF_PART = 1 << 20;
constexpr size_t OFF_CNT  = 2 << 20;
constexpr size_t OFF_XN   = 4u << 20;
constexpr size_t OFF_BOUT = 6u << 20;
constexpr size_t OFF_H    = 8u << 20;
constexpr size_t OFF_QF   = 16u << 20;
constexpr size_t OFF_KF   = 32u << 20;
constexpr size_t OFF_VF   = 48u << 20;
constexpr size_t OFF_OX   = 64u << 20;
constexpr size_t OFF_KMH  = 80u << 20;
constexpr size_t OFF_VMH  = 88u << 20;
constexpr size_t OFF_WOF  = 96u << 20;
constexpr unsigned WS_SPAN = 100u << 20;
constexpr int C_TICKET = 0, C_KV = 1, C_Q = 17, C_H = 33, C_BO = 49, C_WOF = 65, C_X = 66, C_CH = 130;
#define RLX_AGENT __ATOMIC_RELAXED, __HIP_MEMORY_SCOPE_AGENT

__device__ __forceinline__ f32x4 mfma32(bf16x8 a, bf16x8 b, f32x4 c) {
  return __builtin_amdgcn_mfma_f32_16x16x32_bf16(a, b, c, 0, 0, 0);
}
__device__ __forceinline__ f32x4 mfma16(bf16x4 a, bf16x4 b, f32x4 c) {
  bf16x8 a8, b8;
#pragma unroll
  for (int j = 0; j < 4; ++j) { a8[j] = a[j]; b8[j] = b[j]; a8[4 + j] = (__bf16)0.f; b8[4 + j] = (__bf16)0.f; }
  return __builtin_amdgcn_mfma_f32_16x16x32_bf16(a8, b8, c, 0, 0, 0);
}
__device__ __forceinline__ float bfr(float x) { return (float)(__bf16)x; }
__device__ __forceinline__ bf16x4 tr_read(const char* lds_base, unsigned off) {
  s16x4 t = __builtin_amdgcn_ds_read_tr16_b64_v4i16((LAS s16x4*)(lds_base + off));
  return __builtin_bit_cast(bf16x4, t);
}
__device__ __forceinline__ bf16x8 cat(bf16x4 a, bf16x4 b) {
  bf16x8 r; r[0] = a[0]; r[1] = a[1]; r[2] = a[2]; r[3] = a[3]; r[4] = b[0]; r[5] = b[1]; r[6] = b[2]; r[7] = b[3]; return r;
}
__device__ __forceinline__ bf16x4 pk4(f32x4 v) { bf16x4 r; r[0] = (__bf16)v[0]; r[1] = (__bf16)v[1]; r[2] = (__bf16)v[2]; r[3] = (__bf16)v[3]; return r; }

__device__ __forceinline__ float xmax16_32(float x) {
  unsigned u = __builtin_bit_cast(unsigned, x);
  auto a = __builtin_amdgcn_permlane16_swap(u, u, false, false);
  x = fmaxf(__builtin_bit_cast(float, (unsigned)a[0]), __builtin_bit_cast(float, (unsigned)a[1]));
  u = __builtin_bit_cast(unsigned, x);
  auto c = __builtin_amdgcn_permlane32_swap(u, u, false, false);
  return fmaxf(__builtin_bit_cast(float, (unsigned)c[0]), __builtin_bit_cast(float, (unsigned)c[1]));
}
__device__ __forceinline__ float xsum8_16_32(float x) {
  x += __builtin_bit_cast(float, __builtin_amdgcn_update_dpp(0, __builtin_bit_cast(int, x), 0x128, 0xf, 0xf, false));
  unsigned u = __builtin_bit_cast(unsigned, x);
  auto a = __builtin_amdgcn_permlane16_swap(u, u, false, false);
  x = __builtin_bit_cast(float, (unsigned)a[0]) + __builtin_bit_cast(float, (unsigned)a[1]);
  u = __builtin_bit_cast(unsigned, x);
  auto c = __builtin_amdgcn_permlane32_swap(u, u, false, false);
  return __builtin_bit_cast(float, (unsigned)c[0]) + __builtin_bit_cast(float, (unsigned)c[1]);
}

__global__ __launch_bounds__(256) void k_ln(const bf16* __restrict__ x, const bf16* __restrict__ mem0, char* ws) {
  const int lane = threadIdx.x & 63, row = blockIdx.x * 4 + (threadIdx.x >> 6);
  bf16* xn = (bf16*)(ws + OFF_XN);
  bf16x8 v = *(const bf16x8*)(x + (size_t)row * DM + lane * 8);
  float f[8], s = 0.f;
#pragma unroll
  for (int j = 0; j < 8; ++j) { f[j] = (float)v[j]; s += f[j]; }
#pragma unroll
  for (int m = 32; m >= 1; m >>= 1) s += __shfl_xor(s, m);
  const float mean = s * (1.f / DM);
  float q = 0.f;
#pragma unroll
  for (int j = 0; j < 8; ++j) { float d = f[j] - mean; q += d * d; }
#pragma unroll
  for (int m = 32; m >= 1; m >>= 1) q += __shfl_xor(q, m);
  const float mb = bfr(mean), vb = bfr(q * (1.f / DM));
  const float den = bfr(sqrtf(bfr(vb + 1e-5f)));
  bf16x8 o;
#pragma unroll
  for (int j = 0; j < 8; ++j) o[j] = (__bf16)(bfr(f[j] - mb) / den);
  *(bf16x8*)(xn + (size_t)row * DM + lane * 8) = o;
  if (blockIdx.x == 0) {
    for (int c = threadIdx.x; c < 512; c += 256) {
      const int i = c & 7, dg = c >> 3;
      *(bf16x8*)(ws + OFF_MEMH + (size_t)c * 16) = *(const bf16x8*)(mem0 + i * DM + dg * 8);
    }
  }
}

constexpr int GA_PITCH = 144;
constexpr int GA_BYTES = 128 * GA_PITCH;
constexpr int GB_BYTES = 64 * 256;
__device__ __forceinline__ unsigned off_b(unsigned row, unsigned ch) { return 256u * row + 16u * (ch ^ (((row & 3) << 2) | ((row >> 2) & 3))); }
typedef __amdgpu_buffer_rsrc_t rsrc_t;
__device__ __forceinline__ void st16(rsrc_t rs, unsigned off, bf16x8 v) { __builtin_amdgcn_raw_buffer_store_b128(__builtin_bit_cast(u32x4, v), rs, off, 0, 16); }
__device__ __forceinline__ void st8(rsrc_t rs, unsigned off, bf16x4 v) { __builtin_amdgcn_raw_buffer_store_b64(__builtin_bit_cast(u32x2, v), rs, off, 0, 16); }

template <int MODE>
__device__ __forceinline__ void gemm_tile(const bf16* __restrict__ A, int lda, const bf16* __restrict__ B0, const bf16* __restrict__ B1,
                                          int ldb, int K, int row0, int col0, char* lds, rsrc_t rs, unsigned dst) {
  const int tid = threadIdx.x, lane = tid & 63, w = tid >> 6, wm = w >> 1, wn = w & 1;
  const int li = lane & 15, g = lane >> 4, q = li >> 2, p = li & 3;
  char* la = lds; char* lb = lds + GA_BYTES;
  f32x4 acc[2][4];
#pragma unroll
  for (int a = 0; a < 2; ++a)
#pragma unroll
    for (int b = 0; b < 4; ++b) acc[a][b] = (f32x4){0.f, 0.f, 0.f, 0.f};
  const bf16* asrc[2]; const bf16* bsrc[2]; unsigned adst[2], bdst[2];
#pragma unroll
  for (int u = 0; u < 2; ++u) {
    const int c = tid + 512 * u;
    asrc[u] = A + (size_t)(row0 + (c >> 3)) * lda + (c & 7) * 8;
    adst[u] = (c >> 3) * GA_PITCH + (c & 7) * 16;
    const int kr = c >> 4, ch = c & 15;
    if (MODE == 2) {
      const int blk = ch >> 2;
      bsrc[u] = ((blk & 1) ? B1 : B0) + (size_t)kr * ldb + col0 + (blk >> 1) * 32 + (ch & 3) * 8;
    } else {
      bsrc[u] = B0 + (size_t)kr * ldb + col0 + ch * 8;
    }
    bdst[u] = off_b(kr, ch);
  }
  u32x4 ra[2], rb[2];
#pragma unroll
  for (int u = 0; u < 2; ++u) { ra[u] = *(const u32x4*)asrc[u]; rb[u] = *(const u32x4*)bsrc[u]; }
  const int nkt = K >> 6;
  for (int kt = 0; kt < nkt; ++kt) {
#pragma unroll
    for (int u = 0; u < 2; ++u) { *(u32x4*)(la + adst[u]) = ra[u]; *(u32x4*)(lb + bdst[u]) = rb[u]; }
    __syncthreads();
    if (kt + 1 < nkt) {
#pragma unroll
      for (int u = 0; u < 2; ++u) {
        ra[u] = *(const u32x4*)(asrc[u] + (kt + 1) * 64);
        rb[u] = *(const u32x4*)(bsrc[u] + (size_t)(kt + 1) * 64 * ldb);
      }
    }
#pragma unroll
    for (int ks = 0; ks < 2; ++ks) {
      bf16x8 af[2], bfr_[4];
#pragma unroll
      for (int tt = 0; tt < 2; ++tt) af[tt] = *(const bf16x8*)(la + (wm * 32 + tt * 16 + li) * GA_PITCH + ks * 64 + g * 16);
#pragma unroll
      for (int nt = 0; nt < 4; ++nt) {
        const unsigned ch = 2 * (wn * 4 + nt) + (p >> 1);
        bf16x4 lo = tr_read(lb, off_b(32 * ks + 8 * g + q, ch) + 8 * (p & 1));
        bf16x4 hi = tr_read(lb, off_b(32 * ks + 8 * g + 4 + q, ch) + 8 * (p & 1));
        bfr_[nt] = cat(lo, hi);
      }
#pragma unroll
      for (int tt = 0; tt < 2; ++tt)
#pragma unroll
        for (int nt = 0; nt < 4; ++nt) {
          if (MODE == 1) acc[tt][nt] = mfma32(af[tt], bfr_[nt], acc[tt][nt]);
          else acc[tt][nt] = mfma32(bfr_[nt], af[tt], acc[tt][nt]);
        }
    }
    __syncthreads();
  }
  if (MODE == 0) {
    const int seg = (row0 >> 5) + wm, head = col0 >> 7;
#pragma unroll
    for (int tt = 0; tt < 2; ++tt)
#pragma unroll
      for (int k2 = 0; k2 < 2; ++k2)
        st16(rs, dst + ((((seg * NH + head) * 2 + tt) * 4 + (wn * 2 + k2)) * 64 + lane) * 16, cat(pk4(acc[tt][2 * k2]), pk4(acc[tt][2 * k2 + 1])));
  } else if (MODE == 1) {
    const int seg = (row0 >> 5) + wm, head = col0 >> 7;
#pragma unroll
    for (int nt = 0; nt < 4; ++nt)
      st16(rs, dst + (((seg * NH + head) * 8 + (wn * 4 + nt)) * 64 + lane) * 16, cat(pk4(acc[0][nt]), pk4(acc[1][nt])));
  } else if (MODE == 2) {
#pragma unroll
    for (int tt = 0; tt < 2; ++tt)
#pragma unroll
      for (int n2 = 0; n2 < 2; ++n2) {
        f32x4 hv;
#pragma unroll
        for (int r = 0; r < 4; ++r) {
          const float gg = bfr(acc[tt][n2][r]), uu = bfr(acc[tt][n2 + 2][r]);
          const float sg = bfr(1.f / (1.f + __expf(-gg)));
          hv[r] = sg * uu;
        }
        const int row = row0 + wm * 32 + tt * 16 + li, col = col0 + 32 * wn + 16 * n2 + 4 * g;
        st8(rs, dst + (row * FF + col) * 2, pk4(hv));
      }
  } else {
#pragma unroll
    for (int tt = 0; tt < 2; ++tt)
#pragma unroll
      for (int nt = 0; nt < 4; ++nt) {
        const int row = row0 + wm * 32 + tt * 16 + li, col = col0 + 64 * wn + 16 * nt + 4 * g;
        st8(rs, dst + (row * DM + col) * 2, pk4(acc[tt][nt]));
      }
  }
}

__device__ __forceinline__ void xattn_unit(char* ws, rsrc_t rs, int seg, int head) {
  const int lane = threadIdx.x & 63, li = lane & 15, g = lane >> 4;
  const int unit = seg * NH + head;
  const bf16* qf = (const bf16*)(ws + OFF_QF) + (size_t)unit * 4096;
  const bf16* kf = (const bf16*)(ws + OFF_KF) + (size_t)unit * 4096;
  const bf16* vf = (const bf16*)(ws + OFF_VF) + (size_t)unit * 4096;
  const bf16* km = (const bf16*)(ws + OFF_KMH) + (size_t)unit * 2048;
  const bf16* vm = (const bf16*)(ws + OFF_VMH) + (size_t)unit * 2048;
  const float scale = 0.08838834764831845f;
  f32x4 sc[3][2];
#pragma unroll
  for (int a = 0; a < 3; ++a) { sc[a][0] = (f32x4){0, 0, 0, 0}; sc[a][1] = (f32x4){0, 0, 0, 0}; }
#pragma unroll
  for (int kk = 0; kk < 4; ++kk) {
    const bf16x8 q0 = *(const bf16x8*)(qf + (0 * 4 + kk) * 512 + lane * 8), q1 = *(const bf16x8*)(qf + (1 * 4 + kk) * 512 + lane * 8);
    const bf16x8 k0 = *(const bf16x8*)(km + kk * 512 + lane * 8);
    const bf16x8 k1 = *(const bf16x8*)(kf + (0 * 4 + kk) * 512 + lane * 8), k2 = *(const bf16x8*)(kf + (1 * 4 + kk) * 512 + lane * 8);
    sc[0][0] = mfma32(k0, q0, sc[0][0]); sc[0][1] = mfma32(k0, q1, sc[0][1]);
    sc[1][0] = mfma32(k1, q0, sc[1][0]); sc[1][1] = mfma32(k1, q1, sc[1][1]);
    sc[2][0] = mfma32(k2, q0, sc[2][0]); sc[2][1] = mfma32(k2, q1, sc[2][1]);
  }
  bf16x4 p0[2]; bf16x8 p12[2];
#pragma unroll
  for (int qt = 0; qt < 2; ++qt) {
    const int qi = 16 * qt + li;
    float v[12]; float m = -3.0e38f;
#pragma unroll
    for (int kt = 0; kt < 3; ++kt)
#pragma unroll
      for (int r = 0; r < 4; ++r) {
        float s = bfr(bfr(sc[kt][qt][r]) * scale);
        bool ok = (kt == 0) ? (g < 2) : (16 * (kt - 1) + 4 * g + r <= qi);
        s = ok ? s : -3.0e38f;
        v[kt * 4 + r] = s; m = fmaxf(m, s);
      }
    m = xmax16_32(m);
    float sum = 0.f;
#pragma unroll
    for (int e = 0; e < 12; ++e) { v[e] = (v[e] > -1.0e38f) ? __expf(v[e] - m) : 0.f; sum += v[e]; }
    sum += __shfl_xor(sum, 16); sum += __shfl_xor(sum, 32);
    const float inv = 1.f / sum;
#pragma unroll
    for (int r = 0; r < 4; ++r) { p0[qt][r] = (__bf16)(v[r] * inv); p12[qt][r] = (__bf16)(v[4 + r] * inv); p12[qt][4 + r] = (__bf16)(v[8 + r] * inv); }
  }
#pragma unroll
  for (int kk = 0; kk < 4; ++kk) {
    f32x4 o[2][2];
#pragma unroll
    for (int e2 = 0; e2 < 2; ++e2) {
      const int et = 2 * kk + e2;
      const bf16x4 vmf = *(const bf16x4*)(vm + et * 256 + lane * 4);
      const bf16x8 vxf = *(const bf16x8*)(vf + et * 512 + lane * 8);
#pragma unroll
      for (int qt = 0; qt < 2; ++qt) {
        o[qt][e2] = mfma16(vmf, p0[qt], (f32x4){0, 0, 0, 0});
        o[qt][e2] = mfma32(vxf, p12[qt], o[qt][e2]);
      }
    }
#pragma unroll
    for (int qt = 0; qt < 2; ++qt)
      st16(rs, (unsigned)OFF_OX + ((((seg * 2 + qt) * NH + head) * 4 + kk) * 64 + lane) * 16, cat(pk4(o[qt][0]), pk4(o[qt][1])));
  }
}

__device__ __forceinline__ void wof_task(const bf16* __restrict__ Wo, char* lds, rsrc_t rs, int t) {
  const int tid = threadIdx.x, lane = tid & 63, w = tid >> 6, li = lane & 15, g = lane >> 4, q4 = li >> 2, p4 = li & 3;
  const int h = t >> 1, chh = t & 1;
  constexpr int P = 528;
#pragma unroll
  for (int u = 0; u < 8; ++u) {
    const int cidx = tid + 512 * u, rr = cidx >> 5, ch = cidx & 31;
    *(u32x4*)(lds + rr * P + ch * 16) = *(const u32x4*)(Wo + ((size_t)h * DH + rr) * DM + chh * 256 + ch * 8);
  }
  __syncthreads();
#pragma unroll
  for (int n2 = 0; n2 < 2; ++n2)
#pragma unroll
    for (int kk = 0; kk < 4; ++kk) {
      const int ntl = 2 * w + n2;
      bf16x4 lo = tr_read(lds, ((2 * kk + 0) * 16 + 4 * g + q4) * P + (16 * ntl + 4 * p4) * 2);
      bf16x4 hi = tr_read(lds, ((2 * kk + 1) * 16 + 4 * g + q4) * P + (16 * ntl + 4 * p4) * 2);
      st16(rs, (unsigned)OFF_WOF + ((((chh * 16 + ntl) * NH + h) * 4 + kk) * 64 + lane) * 16, cat(lo, hi));
    }
}

__device__ __forceinline__ void final_task(char* ws, char* lds, const bf16* __restrict__ x, bf16* __restrict__ out, int seg, int cb) {
  const int tid = threadIdx.x, lane = tid & 63, w = tid >> 6;
  const bf16* oxf = (const bf16*)(ws + OFF_OX);
  const bf16* wof = (const bf16*)(ws + OFF_WOF);
  f32x4 acc[2][4];
#pragma unroll
  for (int a = 0; a < 2; ++a)
#pragma unroll
    for (int b = 0; b < 4; ++b) acc[a][b] = (f32x4){0.f, 0.f, 0.f, 0.f};
#pragma unroll 4
  for (int i = 0; i < 16; ++i) {
    const int hk = w * 16 + i;
    bf16x8 af[2], bf[4];
#pragma unroll
    for (int tt = 0; tt < 2; ++tt) af[tt] = *(const bf16x8*)(oxf + ((size_t)(seg * 2 + tt) * 128 + hk) * 512 + lane * 8);
#pragma unroll
    for (int nt = 0; nt < 4; ++nt) bf[nt] = *(const bf16x8*)(wof + ((size_t)(cb * 4 + nt) * 128 + hk) * 512 + lane * 8);
#pragma unroll
    for (int tt = 0; tt < 2; ++tt)
#pragma unroll
      for (int nt = 0; nt < 4; ++nt) acc[tt][nt] = mfma32(bf[nt], af[tt], acc[tt][nt]);
  }
#pragma unroll
  for (int tt = 0; tt < 2; ++tt)
#pragma unroll
    for (int nt = 0; nt < 4; ++nt) *(f32x4*)(lds + (((w * 2 + tt) * 4 + nt) * 64 + lane) * 16) = acc[tt][nt];
  __syncthreads();
  {
    const int tt = tid >> 8, nt = (tid >> 6) & 3, li = lane & 15, g = lane >> 4;
    f32x4 a = {0, 0, 0, 0};
#pragma unroll
    for (int ww = 0; ww < 8; ++ww) a += *(const f32x4*)(lds + (((ww * 2 + tt) * 4 + nt) * 64 + lane) * 16);
    const int row = seg * 32 + tt * 16 + li, col = cb * 64 + nt * 16 + 4 * g;
    const bf16x4 xv = *(const bf16x4*)(x + (size_t)row * DM + col);
    const bf16x4 bv = *(const bf16x4*)((const bf16*)(ws + OFF_BOUT) + (size_t)row * DM + col);
    f32x4 o;
#pragma unroll
    for (int r = 0; r < 4; ++r) o[r] = bfr((float)xv[r] + bfr(a[r])) + (float)bv[r];
    *(bf16x4*)(out + (size_t)row * DM + col) = pk4(o);
  }
}

__device__ __forceinline__ unsigned* cnt_ptr(char* ws, int idx) { return (unsigned*)(ws + OFF_CNT) + idx * 64; }
__device__ __forceinline__ void wait_cnt(char* ws, int idx, unsigned target) {
  unsigned spins = 0;
  while (__hip_atomic_load(cnt_ptr(ws, idx), RLX_AGENT) < target) { __builtin_amdgcn_s_sleep(8); if (++spins > (1u << 22)) break; }
}
__device__ __forceinline__ void acquire_all() {
  if (threadIdx.x == 0) { __builtin_amdgcn_fence(__ATOMIC_ACQUIRE, "agent"); asm volatile("s_waitcnt vmcnt(0)" ::: "memory"); }
  __syncthreads();
}
__device__ __forceinline__ void signal_cnt(char* ws, int idx) {
  asm volatile("s_waitcnt vmcnt(0)" ::: "memory");
  __syncthreads();
  if (threadIdx.x == 0) __hip_atomic_fetch_add(cnt_ptr(ws, idx), 1u, RLX_AGENT);
}

constexpr int T_KV = 0, T_Q = T_KV + 1024, T_G = T_Q + 512, T_W = T_G + 448, T_B = T_W + 64, T_S = T_B + 64, T_END = T_S + 64 * 12;

__device__ __forceinline__ void worker(const bf16* __restrict__ x, const bf16* __restrict__ Wq, const bf16* __restrict__ Wk, const bf16* __restrict__ Wv,
                                       const bf16* __restrict__ Wo, const bf16* __restrict__ Wg, const bf16* __restrict__ Wu, const bf16* __restrict__ Wd,
                                       bf16* __restrict__ out, char* ws, char* lds) {
  const rsrc_t rs = __builtin_amdgcn_make_buffer_rsrc(ws, 0, WS_SPAN, 0x00020000);
  const bf16* xn = (const bf16*)(ws + OFF_XN);
  volatile int* tk = (volatile int*)(lds + 144 * 1024);
  for (;;) {
    __syncthreads();
    if (threadIdx.x == 0) *tk = (int)__hip_atomic_fetch_add(cnt_ptr(ws, C_TICKET), 1u, RLX_AGENT);
    __syncthreads();
    const int t = *tk;
    if (t >= T_END) break;
    if (t < T_Q) {
      const int rb = t >> 6, cb = t & 63;
      if (cb < 32) gemm_tile<0>(xn, DM, Wk, nullptr, NH * DH, DM, rb * 128, cb * 128, lds, rs, (unsigned)OFF_KF);
      else gemm_tile<1>(xn, DM, Wv, nullptr, NH * DH, DM, rb * 128, (cb - 32) * 128, lds, rs, (unsigned)OFF_VF);
      signal_cnt(ws, C_KV + rb);
    } else if (t < T_G) {
      const int u = t - T_Q, rb = u >> 5, cb = u & 31;
      gemm_tile<0>(xn, DM, Wq, nullptr, NH * DH, DM, rb * 128, cb * 128, lds, rs, (unsigned)OFF_QF);
      signal_cnt(ws, C_Q + rb);
    } else if (t < T_W) {
      const int u = t - T_G, rb = u / 28, cb = u - rb * 28;
      gemm_tile<2>(xn, DM, Wg, Wu, FF, DM, rb * 128, cb * 64, lds, rs, (unsigned)OFF_H);
      signal_cnt(ws, C_H + rb);
    } else if (t < T_B) {
      wof_task(Wo, lds, rs, t - T_W);
      signal_cnt(ws, C_WOF);
    } else if (t < T_S) {
      const int u = t - T_B, rb = u >> 2, cb = u & 3;
      if (threadIdx.x == 0) wait_cnt(ws, C_H + rb, 28);
      acquire_all();
      gemm_tile<3>((const bf16*)(ws + OFF_H), FF, Wd, nullptr, DM, FF, rb * 128, cb * 128, lds, rs, (unsigned)OFF_BOUT);
      signal_cnt(ws, C_BO + rb);
    } else {
      const int u = t - T_S, seg = u / 12, k = u - seg * 12, rb = seg >> 2;
      if (k < 4) {
        if (threadIdx.x == 0) { wait_cnt(ws, C_Q + rb, 32); wait_cnt(ws, C_KV + rb, 64); }
        if (threadIdx.x < 64) {
          unsigned spins = 0;
          while (!__all(__hip_atomic_load(cnt_ptr(ws, C_CH + seg) + threadIdx.x, RLX_AGENT) != 0u)) { __builtin_amdgcn_s_sleep(16); if (++spins > (1u << 22)) break; }
        }
        acquire_all();
        xattn_unit(ws, rs, seg, k * 8 + (threadIdx.x >> 6));
        signal_cnt(ws, C_X + seg);
      } else {
        if (threadIdx.x == 0) { wait_cnt(ws, C_WOF, 64); wait_cnt(ws, C_BO + rb, 4); wait_cnt(ws, C_X + seg, 4); }
        acquire_all();
        final_task(ws, lds, x, out, seg, k - 4);
      }
    }
  }
}

constexpr int CL_SCR = 0;
constexpr int CL_KX = 0;
constexpr int CL_VX = CL_KX + 8192;
constexpr int CL_MEM = CL_VX + 4096;
constexpr int CL_QF = CL_MEM + 8192;
constexpr int CL_KF = CL_QF + 4096;
constexpr int CL_OF = CL_KF + 4096;
constexpr int CL_P = CL_OF + 2048;
constexpr int CL_WV = 32768;
constexpr int CL_WO = 98304;
constexpr int CHAIN_LDS = 163840;
#define STAMP(k)
constexpr unsigned SENT = 0xFFFFFFFFu;
constexpr unsigned SPIN_CAP = 1u << 22;

__device__ __forceinline__ void chain(const bf16* __restrict__ Wq, const bf16* __restrict__ Wk, const bf16* __restrict__ Wv,
                                      const bf16* __restrict__ Wo, char* ws, char* lds) {
  const int tid = threadIdx.x, lane = tid & 63, w = __builtin_amdgcn_readfirstlane(tid >> 6);
  const int li = lane & 15, g = lane >> 4, q4 = li >> 2, p4 = li & 3;
  const int b = blockIdx.x, h = b >> 1, c = b & 1;
  bf16x8 wqk[2][16];
  {
    constexpr int P1 = 272;
    char* scr = lds + 32768;
#pragma unroll
    for (int it = 0; it < 4; ++it) {
#pragma unroll
      for (int u = 0; u < 8; ++u) {
        const int cidx = tid + 512 * u;
        const int mat = cidx >> 11, rr = (cidx >> 4) & 127, ch = cidx & 15;
        const bf16* src = (mat ? Wk : Wq) + (size_t)(128 * it + rr) * (NH * DH) + h * DH + ch * 8;
        *(u32x4*)(scr + mat * (128 * P1) + rr * P1 + ch * 16) = *(const u32x4*)src;
      }
      __syncthreads();
      const char* base = scr + (w >> 2) * (128 * P1);
#pragma unroll
      for (int k4 = 0; k4 < 4; ++k4)
#pragma unroll
        for (int t = 0; t < 2; ++t) {
          const int c16 = 2 * (w & 3) + t;
          bf16x4 lo = tr_read(base, (32 * k4 + 8 * g + q4) * P1 + (16 * c16 + 4 * p4) * 2);
          bf16x4 hi = tr_read(base, (32 * k4 + 8 * g + 4 + q4) * P1 + (16 * c16 + 4 * p4) * 2);
          wqk[t][4 * it + k4] = cat(lo, hi);
        }
      __syncthreads();
    }
    constexpr int P2 = 1040;
    scr = lds;
#pragma unroll
    for (int u = 0; u < 8; ++u) {
      const int cidx = tid + 512 * u;
      const int rr = cidx >> 6, ch = cidx & 63;
      *(u32x4*)(scr + rr * P2 + ch * 16) = *(const u32x4*)(Wo + ((size_t)h * DH + c * 64 + rr) * DM + ch * 8);
    }
    __syncthreads();
#pragma unroll
    for (int nt = 0; nt < 4; ++nt)
#pragma unroll
      for (int k2 = 0; k2 < 2; ++k2) {
        const int col = 64 * w + 16 * nt + 4 * p4;
        bf16x4 lo = tr_read(scr, ((2 * k2 + 0) * 16 + 4 * g + q4) * P2 + col * 2);
        bf16x4 hi = tr_read(scr, ((2 * k2 + 1) * 16 + 4 * g + q4) * P2 + col * 2);
        *(bf16x8*)(lds + CL_WO + (((w * 4 + nt) * 2 + k2) * 64 + lane) * 16) = cat(lo, hi);
      }
    __syncthreads();
    constexpr int P3 = 144;
#pragma unroll
    for (int it = 0; it < 4; ++it) {
#pragma unroll
      for (int u = 0; u < 2; ++u) {
        const int cidx = tid + 512 * u;
        const int rr = cidx >> 3, ch = cidx & 7;
        *(u32x4*)(scr + rr * P3 + ch * 16) = *(const u32x4*)(Wv + (size_t)(128 * it + rr) * (NH * DH) + h * DH + c * 64 + ch * 8);
      }
      __syncthreads();
#pragma unroll
      for (int e2 = 0; e2 < 2; ++e2) {
        const int et = 2 * (w & 1) + e2, k4 = w >> 1;
        bf16x4 lo = tr_read(scr, (32 * k4 + 8 * g + q4) * P3 + (16 * et + 4 * p4) * 2);
        bf16x4 hi = tr_read(scr, (32 * k4 + 8 * g + 4 + q4) * P3 + (16 * et + 4 * p4) * 2);
        *(bf16x8*)(lds + CL_WV + ((et * 16 + 4 * it + k4) * 64 + lane) * 16) = cat(lo, hi);
      }
      __syncthreads();
    }
  }
  const auto rs_mem = __builtin_amdgcn_make_buffer_rsrc(ws + OFF_MEMH, 0, 65 * 8192, 0x00020000);
  const auto rs_part = __builtin_amdgcn_make_buffer_rsrc(ws + OFF_PART, 0, 1 << 20, 0x00020000);
  const auto rs_km = __builtin_amdgcn_make_buffer_rsrc(ws + OFF_KMH, 0, 16 << 20, 0x00020000);
  const float scale = 0.08838834764831845f;
  const auto rs_kv = __builtin_amdgcn_make_buffer_rsrc(ws + OFF_KF, 0, 32 << 20, 0x00020000);
  unsigned rdy = 0;
  if (w < 4) {
    unsigned spins = 0;
    while (__builtin_amdgcn_readfirstlane(__hip_atomic_load(cnt_ptr(ws, C_KV), RLX_AGENT)) < 64u) { __builtin_amdgcn_s_sleep(4); if (++spins > SPIN_CAP) break; }
    const u32x4 a0 = __builtin_amdgcn_raw_buffer_load_b128(rs_kv, lane * 16, h * 8192 + w * 2048, 16);
    const u32x4 a1 = __builtin_amdgcn_raw_buffer_load_b128(rs_kv, lane * 16, h * 8192 + w * 2048 + 1024, 16);
    const u32x4 a2 = __builtin_amdgcn_raw_buffer_load_b128(rs_kv, lane * 16, (16 << 20) + h * 8192 + c * 4096 + w * 1024, 16);
    *(u32x4*)(lds + lane * 16 + CL_KX + w * 2048) = a0;
    *(u32x4*)(lds + lane * 16 + CL_KX + w * 2048 + 1024) = a1;
    *(u32x4*)(lds + lane * 16 + CL_VX + w * 1024) = a2;
    rdy = 64u;
  }
  const int pst_off = (((8 * w + 2 * (g & 1) + (g >> 1)) * 64 + b) * 8 + li) * 16;
  const char* l16 = lds + lane * 16;
  const char* mfb = lds + CL_MEM + (g * 8 + (li & 7)) * 16;
  const char* wvb = lds + CL_WV + ((w & 3) * 1024 + lane) * 16;
  const char* wob = lds + CL_WO + (w * 512 + lane) * 16;
  for (int s = 0; s < NSEG; ++s) {
    if (w >= 6) {
      u32x4 v[4]; unsigned spins = 0;
      asm volatile("s_waitcnt vmcnt(0)" ::: "memory");
      for (;;) {
        asm volatile("" ::: "memory");
#pragma unroll
        for (int u = 0; u < 4; ++u) v[u] = __builtin_amdgcn_raw_buffer_load_b128(rs_mem, lane * 16, s * 8192 + (w - 6) * 4096 + u * 1024, 16);
        unsigned m = 0;
#pragma unroll
        for (int u = 0; u < 4; ++u) m = max(m, max(v[u][0], v[u][2]));
        if (__all(m != SENT)) break;
        if (++spins > SPIN_CAP) break;
      }
#pragma unroll
      for (int u = 0; u < 4; ++u) *(u32x4*)(l16 + CL_MEM + (w - 6) * 4096 + u * 1024) = v[u];
    }
    STAMP(0);
    __syncthreads();
    STAMP(1);
    if (tid == 256 && s > 0) __hip_atomic_store(cnt_ptr(ws, C_CH + s - 1) + b, 1u, RLX_AGENT);
    u32x4 pf[3];
    const int sn = (s + 1 < NSEG) ? s + 1 : s;
    if (w < 4) {
      if (__builtin_amdgcn_readfirstlane(rdy) < 64u) {
        unsigned spins = 0;
        while (__builtin_amdgcn_readfirstlane(__hip_atomic_load(cnt_ptr(ws, C_KV + (sn >> 2)), RLX_AGENT)) < 64u) { __builtin_amdgcn_s_sleep(2); if (++spins > SPIN_CAP) break; }
      }
      const int so = (sn * NH + h) * 8192;
      pf[0] = __builtin_amdgcn_raw_buffer_load_b128(rs_kv, lane * 16, so + w * 2048, 16);
      pf[1] = __builtin_amdgcn_raw_buffer_load_b128(rs_kv, lane * 16, so + w * 2048 + 1024, 16);
      pf[2] = __builtin_amdgcn_raw_buffer_load_b128(rs_kv, lane * 16, (16 << 20) + so + c * 4096 + w * 1024, 16);
      const int s2 = (s + 2 < NSEG) ? s + 2 : NSEG - 1;
      rdy = __hip_atomic_load(cnt_ptr(ws, C_KV + (s2 >> 2)), RLX_AGENT);
    }
    bf16x8 mf[16];
#pragma unroll
    for (int ks = 0; ks < 16; ++ks) mf[ks] = *(const bf16x8*)(mfb + ks * 512);
    {
      f32x4 a0 = {0, 0, 0, 0}, a1 = {0, 0, 0, 0};
#pragma unroll
      for (int ks = 0; ks < 16; ++ks) {
        a0 = mfma32(wqk[0][ks], mf[ks], a0);
        a1 = mfma32(wqk[1][ks], mf[ks], a1);
      }
      const bf16x8 o = cat(pk4(a0), pk4(a1));
      *(bf16x8*)(lds + (w < 4 ? CL_QF : CL_KF) + ((w & 3) * 64 + lane) * 16) = o;
      if (w >= 4 && c == 0) __builtin_amdgcn_raw_buffer_store_b128(__builtin_bit_cast(u32x4, o), rs_km, lane * 16, ((s * NH + h) * 4 + (w & 3)) * 1024, 16);
    }
    STAMP(2);
    bf16x4 vm4 = {(__bf16)0.f, (__bf16)0.f, (__bf16)0.f, (__bf16)0.f};
    if (w == 0) {
      bf16x8 kxf[8];
#pragma unroll
      for (int u = 0; u < 8; ++u) kxf[u] = *(const bf16x8*)(l16 + CL_KX + u * 1024);
      __syncthreads();
      f32x4 s0 = {0, 0, 0, 0}, s1 = {0, 0, 0, 0}, s2 = {0, 0, 0, 0};
#pragma unroll
      for (int kk = 0; kk < 4; ++kk) {
        const bf16x8 qf = *(const bf16x8*)(l16 + CL_QF + kk * 1024);
        const bf16x8 k0 = *(const bf16x8*)(l16 + CL_KF + kk * 1024);
        s0 = mfma32(k0, qf, s0); s1 = mfma32(kxf[kk], qf, s1); s2 = mfma32(kxf[4 + kk], qf, s2);
      }
      if (g >= 2) s0 = (f32x4){-3.0e38f, -3.0e38f, -3.0e38f, -3.0e38f};
      float m = fmaxf(fmaxf(fmaxf(s0[0], s0[1]), fmaxf(s0[2], s0[3])), fmaxf(fmaxf(fmaxf(s1[0], s1[1]), fmaxf(s1[2], s1[3])), fmaxf(fmaxf(s2[0], s2[1]), fmaxf(s2[2], s2[3]))));
      m = xmax16_32(m);
      const float cexp = scale * 1.4426950408889634f, mc = -m * cexp;
      bf16x4 p0; bf16x8 p12;
#pragma unroll
      for (int r = 0; r < 4; ++r) {
        p0[r] = (__bf16)__builtin_amdgcn_exp2f(fmaf(s0[r], cexp, mc));
        p12[r] = (__bf16)__builtin_amdgcn_exp2f(fmaf(s1[r], cexp, mc));
        p12[4 + r] = (__bf16)__builtin_amdgcn_exp2f(fmaf(s2[r], cexp, mc));
      }
      *(bf16x8*)(lds + CL_P + lane * 16) = p12;
      *(bf16x4*)(lds + CL_P + 1024 + lane * 8) = p0;
    } else {
      if (w >= 4) {
        f32x4 va = {0, 0, 0, 0}, vb = {0, 0, 0, 0};
        bf16x8 wv[2][4];
#pragma unroll
        for (int k1 = 0; k1 < 4; ++k1) wv[0][k1] = *(const bf16x8*)(wvb + k1 * 1024);
        __syncthreads();
#pragma unroll
        for (int k4 = 0; k4 < 4; ++k4) {
          if (k4 < 3) {
#pragma unroll
            for (int k1 = 0; k1 < 4; ++k1) wv[(k4 + 1) & 1][k1] = *(const bf16x8*)(wvb + (4 * k4 + 4 + k1) * 1024);
          }
#pragma unroll
          for (int k1 = 0; k1 < 4; k1 += 2) { va = mfma32(mf[4 * k4 + k1], wv[k4 & 1][k1], va); vb = mfma32(mf[4 * k4 + k1 + 1], wv[k4 & 1][k1 + 1], vb); }
          __builtin_amdgcn_sched_barrier(0);
        }
        vm4 = pk4(va + vb);
        __builtin_amdgcn_raw_buffer_store_b64(__builtin_bit_cast(u32x2, vm4), rs_km, lane * 8, (8 << 20) + ((s * NH + h) * 8 + 4 * c + (w - 4)) * 512, 16);
      } else {
        __syncthreads();
      }
    }
    __syncthreads();
    if (w >= 4) {
      const bf16x8 p12 = *(const bf16x8*)(lds + CL_P + lane * 16);
      const bf16x4 p0 = *(const bf16x4*)(lds + CL_P + 1024 + lane * 8);
      const bf16x8 vx8 = *(const bf16x8*)(lds + CL_VX + ((w - 4) * 64 + lane) * 16);
      f32x4 o = mfma16(vm4, p0, (f32x4){0, 0, 0, 0});
      const bf16x4 one4 = {(__bf16)1.f, (__bf16)1.f, (__bf16)1.f, (__bf16)1.f};
      f32x4 l = mfma16(one4, p0, (f32x4){0, 0, 0, 0});
      o = mfma32(vx8, p12, o);
      l = mfma32(cat(one4, one4), p12, l);
      const float inv = __builtin_amdgcn_rcpf(l[0]);
      *(bf16x4*)(lds + CL_OF + (((w - 4) >> 1) * 64 + lane) * 16 + (w & 1) * 8) = pk4(o * inv);
    }
    bf16x8 wo[4][2];
#pragma unroll
    for (int nt = 0; nt < 4; ++nt)
#pragma unroll
      for (int k2 = 0; k2 < 2; ++k2) wo[nt][k2] = *(const bf16x8*)(wob + (nt * 2 + k2) * 1024);
    STAMP(3);
    __syncthreads();
    STAMP(4);
    if (s + 1 == NSEG) {
      if (w >= 4) asm volatile("s_waitcnt vmcnt(0)" ::: "memory");
      __syncthreads();
      if (tid == 64) __hip_atomic_store(cnt_ptr(ws, C_CH + s) + b, 1u, RLX_AGENT);
      break;
    }
    const int slot = (s & 1) << 19;
    {
      const bf16x8 of0 = *(const bf16x8*)(l16 + CL_OF);
      const bf16x8 of1 = *(const bf16x8*)(l16 + CL_OF + 1024);
#pragma unroll
      for (int n2 = 0; n2 < 2; ++n2) {
        f32x4 pa = mfma32(wo[2 * n2][0], of0, (f32x4){0, 0, 0, 0});
        f32x4 pb = mfma32(wo[2 * n2 + 1][0], of0, (f32x4){0, 0, 0, 0});
        pa = mfma32(wo[2 * n2][1], of1, pa);
        pb = mfma32(wo[2 * n2 + 1][1], of1, pb);
        u32x2 A = __builtin_bit_cast(u32x2, pk4(pa)), B = __builtin_bit_cast(u32x2, pk4(pb));
        auto r0 = __builtin_amdgcn_permlane16_swap(A[0], B[0], false, false);
        auto r1 = __builtin_amdgcn_permlane16_swap(A[1], B[1], false, false);
        u32x4 pv = {r0[0] | 1u, r1[0], r0[1] | 1u, r1[1]};
        if (li < 8) __builtin_amdgcn_raw_buffer_store_b128(pv, rs_part, pst_off, slot + n2 * 32768, 16);
      }
    }
    STAMP(5);
    if (w < 4) {
      *(u32x4*)(l16 + CL_KX + w * 2048) = pf[0];
      *(u32x4*)(l16 + CL_KX + w * 2048 + 1024) = pf[1];
      *(u32x4*)(l16 + CL_VX + w * 1024) = pf[2];
    }
    if (w == 4 || w == 5) asm volatile("s_waitcnt vmcnt(0)" ::: "memory");
    if (w >= 6) {
      u32x2 v[8]; unsigned spins = 0;
      const int roff = lane * 16 + (w - 6) * 8;
      asm volatile("s_waitcnt vmcnt(0)" ::: "memory");
      for (;;) {
        asm volatile("" ::: "memory");
#pragma unroll
        for (int it = 0; it < 8; ++it) v[it] = __builtin_amdgcn_raw_buffer_load_b64(rs_part, roff, slot + b * 8192 + it * 1024, 16);
        unsigned m = 1u;
#pragma unroll
        for (int it = 0; it < 8; ++it) m &= v[it][0];
        if (__all(m != 0u)) break;
        if (++spins > SPIN_CAP) break;
      }
      STAMP(6);
      f32x4 a = {0, 0, 0, 0};
#pragma unroll
      for (int it = 0; it < 8; ++it) {
        a[0] += __builtin_bit_cast(float, v[it][0] << 16); a[1] += __builtin_bit_cast(float, v[it][0] & 0xffff0000u);
        a[2] += __builtin_bit_cast(float, v[it][1] << 16); a[3] += __builtin_bit_cast(float, v[it][1] & 0xffff0000u);
      }
#pragma unroll
      for (int r = 0; r < 4; ++r) a[r] = xsum8_16_32(a[r]);
      u32x2 o = __builtin_bit_cast(u32x2, pk4(a));
      if (o[0] == SENT) o[0] = 0x7FFF7FFFu;
      if (lane < 8) __builtin_amdgcn_raw_buffer_store_b64(o, rs_mem, lane * 16 + (w - 6) * 8, (s + 1) * 8192 + b * 128, 16);
      const u32x2 z = {0u, 0u};
#pragma unroll
      for (int it = 0; it < 8; ++it) __builtin_amdgcn_raw_buffer_store_b64(z, rs_part, roff, slot + b * 8192 + it * 1024, 16);
      STAMP(7);
    }
  }
}

__global__ __launch_bounds__(512, 2) void k_main(const bf16* __restrict__ x, const bf16* __restrict__ Wq, const bf16* __restrict__ Wk,
                                                 const bf16* __restrict__ Wv, const bf16* __restrict__ Wo, const bf16* __restrict__ Wg,
                                                 const bf16* __restrict__ Wu, const bf16* __restrict__ Wd, bf16* __restrict__ out, char* ws) {
  extern __shared__ __attribute__((aligned(16))) char lds[];
  if (blockIdx.x < 64) chain(Wq, Wk, Wv, Wo, ws, lds);
  else worker(x, Wq, Wk, Wv, Wo, Wg, Wu, Wd, out, ws, lds);
}

extern "C" void kernel_launch(void* const* d_in, const int* in_sizes, int n_in,
                              void* d_out, int out_size, void* d_ws, size_t ws_size,
                              hipStream_t stream) {
  const bf16* x = (const bf16*)d_in[0];
  const bf16* mem0 = (const bf16*)d_in[1];
  const bf16* Wq = (const bf16*)d_in[2];
  const bf16* Wk = (const bf16*)d_in[3];
  const bf16* Wv = (const bf16*)d_in[4];
  const bf16* Wo = (const bf16*)d_in[5];
  const bf16* Wg = (const bf16*)d_in[8];
  const bf16* Wu = (const bf16*)d_in[9];
  const bf16* Wd = (const bf16*)d_in[10];
  char* ws = (char*)d_ws;
  (void)hipFuncSetAttribute((const void*)k_main, hipFuncAttributeMaxDynamicSharedMemorySize, CHAIN_LDS);
  (void)hipMemsetAsync(ws + OFF_MEMH, 0xFF, 65 * 8192, stream);
  (void)hipMemsetAsync(ws + OFF_PART, 0, (1 << 20) + 65536, stream);
  k_ln<<<S_ / 4, 256, 0, stream>>>(x, mem0, ws);
  k_main<<<256, 512, CHAIN_LDS, stream>>>(x, Wq, Wk, Wv, Wo, Wg, Wu, Wd, (bf16*)d_out, ws);
}
```

```cpp
#include <hip/hip_runtime.h>
#include <hip/hip_bf16.h>

constexpr int S_ = 2048, DM = 512, NH = 32, DH = 128, MM = 8, SEG = 32, NSEG = 64, FF = 1792;

typedef __bf16 bf16;
typedef __bf16 bf16x8 __attribute__((ext_vector_type(8)));
typedef __bf16 bf16x4 __attribute__((ext_vector_type(4)));
typedef short s16x4 __attribute__((ext_vector_type(4)));
typedef float f32x4 __attribute__((ext_vector_type(4)));
typedef unsigned u32x4 __attribute__((ext_vector_type(4)));
typedef unsigned u32x2 __attribute__((ext_vector_type(2)));
#define LAS __attribute__((address_space(3)))

constexpr size_t OFF_MEMH = 0;
constexpr size_t OFF_PART = 1 << 20;
constexpr size_t OFF_CNT  = 2 << 20;
constexpr size_t OFF_XN   = 4u << 20;
constexpr size_t OFF_BOUT = 6u << 20;
constexpr size_t OFF_H    = 8u << 20;
constexpr size_t OFF_QF   = 16u << 20;
constexpr size_t OFF_KF   = 32u << 20;
constexpr size_t OFF_VF   = 48u << 20;
constexpr size_t OFF_OX   = 64u << 20;
constexpr size_t OFF_KMH  = 80u << 20;
constexpr size_t OFF_VMH  = 88u << 20;
constexpr size_t OFF_WOF  = 96u << 20;
constexpr unsigned WS_SPAN = 100u << 20;
constexpr int C_TICKET = 0, C_KV = 1, C_Q = 17, C_H = 33, C_BO = 49, C_WOF = 65, C_X = 66, C_CH = 130;
#define RLX_AGENT __ATOMIC_RELAXED, __HIP_MEMORY_SCOPE_AGENT

__device__ __forceinline__ f32x4 mfma32(bf16x8 a, bf16x8 b, f32x4 c) {
  return __builtin_amdgcn_mfma_f32_16x16x32_bf16(a, b, c, 0, 0, 0);
}
__device__ __forceinline__ f32x4 mfma16(bf16x4 a, bf16x4 b, f32x4 c) {
  bf16x8 a8, b8;
#pragma unroll
  for (int j = 0; j < 4; ++j) { a8[j] = a[j]; b8[j] = b[j]; a8[4 + j] = (__bf16)0.f; b8[4 + j] = (__bf16)0.f; }
  return __builtin_amdgcn_mfma_f32_16x16x32_bf16(a8, b8, c, 0, 0, 0);
}
__device__ __forceinline__ float bfr(float x) { return (float)(__bf16)x; }
__device__ __forceinline__ bf16x4 tr_read(const char* lds_base, unsigned off) {
  s16x4 t = __builtin_amdgcn_ds_read_tr16_b64_v4i16((LAS s16x4*)(lds_base + off));
  return __builtin_bit_cast(bf16x4, t);
}
__device__ __forceinline__ bf16x8 cat(bf16x4 a, bf16x4 b) {
  bf16x8 r; r[0] = a[0]; r[1] = a[1]; r[2] = a[2]; r[3] = a[3]; r[4] = b[0]; r[5] = b[1]; r[6] = b[2]; r[7] = b[3]; return r;
}
__device__ __forceinline__ bf16x4 pk4(f32x4 v) { bf16x4 r; r[0] = (__bf16)v[0]; r[1] = (__bf16)v[1]; r[2] = (__bf16)v[2]; r[3] = (__bf16)v[3]; return r; }

__device__ __forceinline__ float xmax16_32(float x) {
  unsigned u = __builtin_bit_cast(unsigned, x);
  auto a = __builtin_amdgcn_permlane16_swap(u, u, false, false);
  x = fmaxf(__builtin_bit_cast(float, (unsigned)a[0]), __builtin_bit_cast(float, (unsigned)a[1]));
  u = __builtin_bit_cast(unsigned, x);
  auto c = __builtin_amdgcn_permlane32_swap(u, u, false, false);
  return fmaxf(__builtin_bit_cast(float, (unsigned)c[0]), __builtin_bit_cast(float, (unsigned)c[1]));
}
__device__ __forceinline__ float xsum8_16_32(float x) {
  x += __builtin_bit_cast(float, __builtin_amdgcn_update_dpp(0, __builtin_bit_cast(int, x), 0x128, 0xf, 0xf, false));
  unsigned u = __builtin_bit_cast(unsigned, x);
  auto a = __builtin_amdgcn_permlane16_swap(u, u, false, false);
  x = __builtin_bit_cast(float, (unsigned)a[0]) + __builtin_bit_cast(float, (unsigned)a[1]);
  u = __builtin_bit_cast(unsigned, x);
  auto c = __builtin_amdgcn_permlane32_swap(u, u, false, false);
  return __builtin_bit_cast(float, (unsigned)c[0]) + __builtin_bit_cast(float, (unsigned)c[1]);
}

__global__ __launch_bounds__(256) void k_ln(const bf16* __restrict__ x, const bf16* __restrict__ mem0, char* ws) {
  const int lane = threadIdx.x & 63, row = blockIdx.x * 4 + (threadIdx.x >> 6);
  bf16* xn = (bf16*)(ws + OFF_XN);
  bf16x8 v = *(const bf16x8*)(x + (size_t)row * DM + lane * 8);
  float f[8], s = 0.f;
#pragma unroll
  for (int j = 0; j < 8; ++j) { f[j] = (float)v[j]; s += f[j]; }
#pragma unroll
  for (int m = 32; m >= 1; m >>= 1) s += __shfl_xor(s, m);
  const float mean = s * (1.f / DM);
  float q = 0.f;
#pragma unroll
  for (int j = 0; j < 8; ++j) { float d = f[j] - mean; q += d * d; }
#pragma unroll
  for (int m = 32; m >= 1; m >>= 1) q += __shfl_xor(q, m);
  const float mb = bfr(mean), vb = bfr(q * (1.f / DM));
  const float den = bfr(sqrtf(bfr(vb + 1e-5f)));
  bf16x8 o;
#pragma unroll
  for (int j = 0; j < 8; ++j) o[j] = (__bf16)(bfr(f[j] - mb) / den);
  *(bf16x8*)(xn + (size_t)row * DM + lane * 8) = o;
  {
    const u32x4 ff = {0xFFFFFFFFu, 0xFFFFFFFFu, 0xFFFFFFFFu, 0xFFFFFFFFu}, zz = {0u, 0u, 0u, 0u};
    if (threadIdx.x < 64) *(u32x4*)(ws + OFF_MEMH + 8192 + ((size_t)blockIdx.x * 64 + threadIdx.x) * 16) = ff;
    if (threadIdx.x < 136) *(u32x4*)(ws + OFF_PART + ((size_t)blockIdx.x * 136 + threadIdx.x) * 16) = zz;
  }
  if (blockIdx.x == 0) {
    for (int c = threadIdx.x; c < 512; c += 256) {
      const int i = c & 7, dg = c >> 3;
      *(bf16x8*)(ws + OFF_MEMH + (size_t)c * 16) = *(const bf16x8*)(mem0 + i * DM + dg * 8);
    }
  }
}

constexpr int GA_PITCH = 272;
constexpr int GA_BYTES = 128 * GA_PITCH;
constexpr int GB_BYTES = 128 * 256;
__device__ __forceinline__ unsigned off_b(unsigned row, unsigned ch) { return 256u * row + 16u * (ch ^ (((row & 3) << 2) | ((row >> 2) & 3))); }
typedef __amdgpu_buffer_rsrc_t rsrc_t;
__device__ __forceinline__ void st16(rsrc_t rs, unsigned off, bf16x8 v) { __builtin_amdgcn_raw_buffer_store_b128(__builtin_bit_cast(u32x4, v), rs, off, 0, 16); }
__device__ __forceinline__ void st8(rsrc_t rs, unsigned off, bf16x4 v) { __builtin_amdgcn_raw_buffer_store_b64(__builtin_bit_cast(u32x2, v), rs, off, 0, 16); }

__device__ __forceinline__ bf16x8 ld16(rsrc_t rs, unsigned voff, unsigned soff) { return __builtin_bit_cast(bf16x8, __builtin_amdgcn_raw_buffer_load_b128(rs, voff, soff, 16)); }
__device__ __forceinline__ bf16x4 ld8(rsrc_t rs, unsigned voff, unsigned soff) { return __builtin_bit_cast(bf16x4, __builtin_amdgcn_raw_buffer_load_b64(rs, voff, soff, 16)); }

template <int MODE>
__device__ __forceinline__ void gemm_tile(const bf16* __restrict__ A, int lda, const bf16* __restrict__ B0, const bf16* __restrict__ B1,
                                          int ldb, int K, int row0, int col0, char* lds, rsrc_t rs, unsigned dst, int tid) {
  const int lane = tid & 63, w = tid >> 6, wm = w >> 1, wn = w & 1;
  const int li = lane & 15, g = lane >> 4, q = li >> 2, p = li & 3;
  char* la = lds; char* lb = lds + GA_BYTES;
  f32x4 acc[2][4];
#pragma unroll
  for (int a = 0; a < 2; ++a)
#pragma unroll
    for (int b = 0; b < 4; ++b) acc[a][b] = (f32x4){0.f, 0.f, 0.f, 0.f};
  const bf16* asrc[4]; const bf16* bsrc[4]; unsigned adst[4], bdst[4];
#pragma unroll
  for (int u = 0; u < 4; ++u) {
    const int c = tid + 512 * u;
    asrc[u] = A + (size_t)(row0 + (c >> 4)) * lda + (c & 15) * 8;
    adst[u] = (c >> 4) * GA_PITCH + (c & 15) * 16;
    const int kr = c >> 4, ch = c & 15;
    if (MODE == 2) {
      const int blk = ch >> 2;
      bsrc[u] = ((blk & 1) ? B1 : B0) + (size_t)kr * ldb + col0 + (blk >> 1) * 32 + (ch & 3) * 8;
    } else {
      bsrc[u] = B0 + (size_t)kr * ldb + col0 + ch * 8;
    }
    bdst[u] = off_b(kr, ch);
  }
  u32x4 ra[4], rb[4];
#pragma unroll
  for (int u = 0; u < 4; ++u) { ra[u] = *(const u32x4*)asrc[u]; rb[u] = *(const u32x4*)bsrc[u]; }
  const int nkt = K >> 7;
  for (int kt = 0; kt < nkt; ++kt) {
#pragma unroll
    for (int u = 0; u < 4; ++u) { *(u32x4*)(la + adst[u]) = ra[u]; *(u32x4*)(lb + bdst[u]) = rb[u]; }
    __syncthreads();
    if (kt + 1 < nkt) {
#pragma unroll
      for (int u = 0; u < 4; ++u) {
        ra[u] = *(const u32x4*)(asrc[u] + (kt + 1) * 128);
        rb[u] = *(const u32x4*)(bsrc[u] + (size_t)(kt + 1) * 128 * ldb);
      }
    }
#pragma unroll
    for (int ks = 0; ks < 4; ++ks) {
      bf16x8 af[2], bfr_[4];
#pragma unroll
      for (int tt = 0; tt < 2; ++tt) af[tt] = *(const bf16x8*)(la + (wm * 32 + tt * 16 + li) * GA_PITCH + ks * 64 + g * 16);
#pragma unroll
      for (int nt = 0; nt < 4; ++nt) {
        const unsigned ch = 2 * (wn * 4 + nt) + (p >> 1);
        bf16x4 lo = tr_read(lb, off_b(32 * ks + 8 * g + q, ch) + 8 * (p & 1));
        bf16x4 hi = tr_read(lb, off_b(32 * ks + 8 * g + 4 + q, ch) + 8 * (p & 1));
        bfr_[nt] = cat(lo, hi);
      }
#pragma unroll
      for (int tt = 0; tt < 2; ++tt)
#pragma unroll
        for (int nt = 0; nt < 4; ++nt) {
          if (MODE == 1) acc[tt][nt] = mfma32(af[tt], bfr_[nt], acc[tt][nt]);
          else acc[tt][nt] = mfma32(bfr_[nt], af[tt], acc[tt][nt]);
        }
    }
    __syncthreads();
  }
  if (MODE == 0) {
    const int seg = (row0 >> 5) + wm, head = col0 >> 7;
#pragma unroll
    for (int tt = 0; tt < 2; ++tt)
#pragma unroll
      for (int k2 = 0; k2 < 2; ++k2)
        st16(rs, dst + ((((seg * NH + head) * 2 + tt) * 4 + (wn * 2 + k2)) * 64 + lane) * 16, cat(pk4(acc[tt][2 * k2]), pk4(acc[tt][2 * k2 + 1])));
  } else if (MODE == 1) {
    const int seg = (row0 >> 5) + wm, head = col0 >> 7;
#pragma unroll
    for (int nt = 0; nt < 4; ++nt)
      st16(rs, dst + (((seg * NH + head) * 8 + (wn * 4 + nt)) * 64 + lane) * 16, cat(pk4(acc[0][nt]), pk4(acc[1][nt])));
  } else if (MODE == 2) {
#pragma unroll
    for (int tt = 0; tt < 2; ++tt)
#pragma unroll
      for (int n2 = 0; n2 < 2; ++n2) {
        f32x4 hv;
#pragma unroll
        for (int r = 0; r < 4; ++r) {
          const float gg = bfr(acc[tt][n2][r]), uu = bfr(acc[tt][n2 + 2][r]);
          const float sg = bfr(1.f / (1.f + __expf(-gg)));
          hv[r] = sg * uu;
        }
        const int row = row0 + wm * 32 + tt * 16 + li, col = col0 + 32 * wn + 16 * n2 + 4 * g;
        st8(rs, dst + (row * FF + col) * 2, pk4(hv));
      }
  } else {
#pragma unroll
    for (int tt = 0; tt < 2; ++tt)
#pragma unroll
      for (int nt = 0; nt < 4; ++nt) {
        const int row = row0 + wm * 32 + tt * 16 + li, col = col0 + 64 * wn + 16 * nt + 4 * g;
        st8(rs, dst + (row * DM + col) * 2, pk4(acc[tt][nt]));
      }
  }
}

__device__ __forceinline__ unsigned* cnt_ptr(char* ws, int idx) { return (unsigned*)(ws + OFF_CNT) + idx * 64; }
__device__ __forceinline__ void wait_cnt(char* ws, int idx, unsigned target) {
  unsigned spins = 0;
  while (__hip_atomic_load(cnt_ptr(ws, idx), RLX_AGENT) < target) { __builtin_amdgcn_s_sleep(4); if (++spins > (1u << 22)) break; }
}
__device__ __forceinline__ void acquire_all() {
  if (threadIdx.x == 0) { __builtin_amdgcn_fence(__ATOMIC_ACQUIRE, "agent"); asm volatile("s_waitcnt vmcnt(0)" ::: "memory"); }
  __syncthreads();
}
__device__ __forceinline__ void signal_cnt(char* ws, int idx) {
  asm volatile("s_waitcnt vmcnt(0)" ::: "memory");
  __syncthreads();
  if (threadIdx.x == 0) __hip_atomic_fetch_add(cnt_ptr(ws, idx), 1u, RLX_AGENT);
}

__device__ __forceinline__ void xattn_task(char* ws, rsrc_t rs, int seg, int head, int tid) {
  const int lane = tid & 63, li = lane & 15, g = lane >> 4;
  const int unit = seg * NH + head, rb = seg >> 2;
  const unsigned qf = (unsigned)OFF_QF + unit * 8192, kf = (unsigned)OFF_KF + unit * 8192, vf = (unsigned)OFF_VF + unit * 8192;
  const unsigned km = (unsigned)OFF_KMH + unit * 4096, vm = (unsigned)OFF_VMH + unit * 4096;
  const float scale = 0.08838834764831845f;
  if (threadIdx.x == 0) { wait_cnt(ws, C_Q + rb, 32); wait_cnt(ws, C_KV + rb, 64); }
  __syncthreads();
  f32x4 sc[3][2];
#pragma unroll
  for (int a = 0; a < 3; ++a) { sc[a][0] = (f32x4){0, 0, 0, 0}; sc[a][1] = (f32x4){0, 0, 0, 0}; }
  bf16x8 q[2][4], vx[8];
#pragma unroll
  for (int kk = 0; kk < 4; ++kk) {
    q[0][kk] = ld16(rs, lane * 16, qf + kk * 1024); q[1][kk] = ld16(rs, lane * 16, qf + 4096 + kk * 1024);
    const bf16x8 k1 = ld16(rs, lane * 16, kf + kk * 1024), k2 = ld16(rs, lane * 16, kf + 4096 + kk * 1024);
    sc[1][0] = mfma32(k1, q[0][kk], sc[1][0]); sc[1][1] = mfma32(k1, q[1][kk], sc[1][1]);
    sc[2][0] = mfma32(k2, q[0][kk], sc[2][0]); sc[2][1] = mfma32(k2, q[1][kk], sc[2][1]);
  }
#pragma unroll
  for (int et = 0; et < 8; ++et) vx[et] = ld16(rs, lane * 16, vf + et * 1024);
  if (threadIdx.x < 64) {
    unsigned spins = 0;
    while (!__all(__hip_atomic_load(cnt_ptr(ws, C_CH + seg) + threadIdx.x, RLX_AGENT) != 0u)) { __builtin_amdgcn_s_sleep(4); if (++spins > (1u << 22)) break; }
  }
  __syncthreads();
  bf16x8 k0[4]; bf16x4 vmf[8];
#pragma unroll
  for (int kk = 0; kk < 4; ++kk) k0[kk] = ld16(rs, lane * 16, km + kk * 1024);
#pragma unroll
  for (int et = 0; et < 8; ++et) vmf[et] = ld8(rs, lane * 8, vm + et * 512);
#pragma unroll
  for (int kk = 0; kk < 4; ++kk) { sc[0][0] = mfma32(k0[kk], q[0][kk], sc[0][0]); sc[0][1] = mfma32(k0[kk], q[1][kk], sc[0][1]); }
  bf16x4 p0[2]; bf16x8 p12[2];
#pragma unroll
  for (int qt = 0; qt < 2; ++qt) {
    const int qi = 16 * qt + li;
    float v[12]; float m = -3.0e38f;
#pragma unroll
    for (int kt = 0; kt < 3; ++kt)
#pragma unroll
      for (int r = 0; r < 4; ++r) {
        float s = bfr(bfr(sc[kt][qt][r]) * scale);
        bool ok = (kt == 0) ? (g < 2) : (16 * (kt - 1) + 4 * g + r <= qi);
        s = ok ? s : -3.0e38f;
        v[kt * 4 + r] = s; m = fmaxf(m, s);
      }
    m = xmax16_32(m);
    float sum = 0.f;
#pragma unroll
    for (int e = 0; e < 12; ++e) { v[e] = (v[e] > -1.0e38f) ? __expf(v[e] - m) : 0.f; sum += v[e]; }
    sum += __shfl_xor(sum, 16); sum += __shfl_xor(sum, 32);
    const float inv = 1.f / sum;
#pragma unroll
    for (int r = 0; r < 4; ++r) { p0[qt][r] = (__bf16)(v[r] * inv); p12[qt][r] = (__bf16)(v[4 + r] * inv); p12[qt][4 + r] = (__bf16)(v[8 + r] * inv); }
  }
#pragma unroll
  for (int kk = 0; kk < 4; ++kk) {
    f32x4 o[2][2];
#pragma unroll
    for (int e2 = 0; e2 < 2; ++e2)
#pragma unroll
      for (int qt = 0; qt < 2; ++qt) {
        o[qt][e2] = mfma16(vmf[2 * kk + e2], p0[qt], (f32x4){0, 0, 0, 0});
        o[qt][e2] = mfma32(vx[2 * kk + e2], p12[qt], o[qt][e2]);
      }
#pragma unroll
    for (int qt = 0; qt < 2; ++qt)
      st16(rs, (unsigned)OFF_OX + ((((seg * 2 + qt) * NH + head) * 4 + kk) * 64 + lane) * 16, cat(pk4(o[qt][0]), pk4(o[qt][1])));
  }
  signal_cnt(ws, C_X + seg);
}

__device__ __forceinline__ void wof_task(const bf16* __restrict__ Wo, char* lds, rsrc_t rs, int t, int tid) {
  const int lane = tid & 63, w = tid >> 6, li = lane & 15, g = lane >> 4, q4 = li >> 2, p4 = li & 3;
  const int h = t >> 1, chh = t & 1;
  constexpr int P = 528;
#pragma unroll
  for (int u = 0; u < 8; ++u) {
    const int cidx = tid + 512 * u, rr = cidx >> 5, ch = cidx & 31;
    *(u32x4*)(lds + rr * P + ch * 16) = *(const u32x4*)(Wo + ((size_t)h * DH + rr) * DM + chh * 256 + ch * 8);
  }
  __syncthreads();
#pragma unroll
  for (int n2 = 0; n2 < 2; ++n2)
#pragma unroll
    for (int kk = 0; kk < 4; ++kk) {
      const int ntl = 2 * w + n2;
      bf16x4 lo = tr_read(lds, ((2 * kk + 0) * 16 + 4 * g + q4) * P + (16 * ntl + 4 * p4) * 2);
      bf16x4 hi = tr_read(lds, ((2 * kk + 1) * 16 + 4 * g + q4) * P + (16 * ntl + 4 * p4) * 2);
      st16(rs, (unsigned)OFF_WOF + ((((chh * 16 + ntl) * NH + h) * 4 + kk) * 64 + lane) * 16, cat(lo, hi));
    }
}

__device__ __forceinline__ void final_task(char* ws, rsrc_t rs, char* lds, const bf16* __restrict__ x, bf16* __restrict__ out, int seg, int cb, int tid) {
  const int lane = tid & 63, w = __builtin_amdgcn_readfirstlane(tid >> 6);
  if (tid == 0) { wait_cnt(ws, C_WOF, 64); wait_cnt(ws, C_BO + (seg >> 2), 4); }
  __syncthreads();
  bf16x8 bf[16][2];
#pragma unroll
  for (int i = 0; i < 16; ++i)
#pragma unroll
    for (int nt = 0; nt < 2; ++nt) bf[i][nt] = ld16(rs, lane * 16, (unsigned)OFF_WOF + ((cb * 2 + nt) * 128 + w * 16 + i) * 1024);
  const int tt_ = (tid >> 7) & 1, nt_ = (tid >> 6) & 1, li = lane & 15, g = lane >> 4;
  const int row = seg * 32 + tt_ * 16 + li, col = cb * 32 + nt_ * 16 + 4 * g;
  const bf16x4 xv = *(const bf16x4*)(x + (size_t)row * DM + col);
  const bf16x4 bv = ld8(rs, (row * DM + col) * 2, (unsigned)OFF_BOUT);
  if (tid == 0) wait_cnt(ws, C_X + seg, 4);
  __syncthreads();
  f32x4 acc[2][2];
#pragma unroll
  for (int a = 0; a < 2; ++a) { acc[a][0] = (f32x4){0.f, 0.f, 0.f, 0.f}; acc[a][1] = (f32x4){0.f, 0.f, 0.f, 0.f}; }
#pragma unroll
  for (int i4 = 0; i4 < 4; ++i4) {
    bf16x8 af[4][2];
#pragma unroll
    for (int i = 0; i < 4; ++i)
#pragma unroll
      for (int tt = 0; tt < 2; ++tt) af[i][tt] = ld16(rs, lane * 16, (unsigned)OFF_OX + ((seg * 2 + tt) * 128 + w * 16 + i4 * 4 + i) * 1024);
#pragma unroll
    for (int i = 0; i < 4; ++i)
#pragma unroll
      for (int tt = 0; tt < 2; ++tt)
#pragma unroll
        for (int nt = 0; nt < 2; ++nt) acc[tt][nt] = mfma32(bf[i4 * 4 + i][nt], af[i][tt], acc[tt][nt]);
    if (i4 == 1) __builtin_amdgcn_sched_barrier(0);
  }
#pragma unroll
  for (int tt = 0; tt < 2; ++tt)
#pragma unroll
    for (int nt = 0; nt < 2; ++nt) *(f32x4*)(lds + (((w * 2 + tt) * 2 + nt) * 64 + lane) * 16) = acc[tt][nt];
  __syncthreads();
  if (tid < 256) {
    f32x4 a = {0, 0, 0, 0};
#pragma unroll
    for (int ww = 0; ww < 8; ++ww) a += *(const f32x4*)(lds + (((ww * 2 + tt_) * 2 + nt_) * 64 + lane) * 16);
    f32x4 o;
#pragma unroll
    for (int r = 0; r < 4; ++r) o[r] = bfr((float)xv[r] + bfr(a[r])) + (float)bv[r];
    *(bf16x4*)(out + (size_t)row * DM + col) = pk4(o);
  }
}

constexpr int T_KV = 0, T_Q = T_KV + 1024, T_G = T_Q + 512, T_W = T_G + 448, T_B = T_W + 64, T_S = T_B + 64, T_END = T_S + 64 * 20;

__device__ __forceinline__ void worker(const bf16* __restrict__ x, const bf16* __restrict__ Wq, const bf16* __restrict__ Wk, const bf16* __restrict__ Wv,
                                       const bf16* __restrict__ Wo, const bf16* __restrict__ Wg, const bf16* __restrict__ Wu, const bf16* __restrict__ Wd,
                                       bf16* __restrict__ out, char* ws, char* lds) {
  const rsrc_t rs = __builtin_amdgcn_make_buffer_rsrc(ws, 0, WS_SPAN, 0x00020000);
  const bf16* xn = (const bf16*)(ws + OFF_XN);
  volatile int* tk = (volatile int*)(lds + 144 * 1024);
  for (;;) {
    __syncthreads();
    if (threadIdx.x == 0) *tk = (int)__hip_atomic_fetch_add(cnt_ptr(ws, C_TICKET), 1u, RLX_AGENT);
    __syncthreads();
    const int t = *tk;
    int tid = threadIdx.x; asm volatile("" : "+v"(tid));
    if (t >= T_END) break;
    if (t >= 64 && t < 192) { for (int d = 0; d < 150; ++d) __builtin_amdgcn_s_sleep(1); }
    if (t < T_Q) {
      const int rb = t >> 6, cb = t & 63;
      if (cb < 32) gemm_tile<0>(xn, DM, Wk, nullptr, NH * DH, DM, rb * 128, cb * 128, lds, rs, (unsigned)OFF_KF, tid);
      else gemm_tile<1>(xn, DM, Wv, nullptr, NH * DH, DM, rb * 128, (cb - 32) * 128, lds, rs, (unsigned)OFF_VF, tid);
      signal_cnt(ws, C_KV + rb);
    } else if (t < T_G) {
      const int u = t - T_Q, rb = u >> 5, cb = u & 31;
      gemm_tile<0>(xn, DM, Wq, nullptr, NH * DH, DM, rb * 128, cb * 128, lds, rs, (unsigned)OFF_QF, tid);
      signal_cnt(ws, C_Q + rb);
    } else if (t < T_W) {
      const int u = t - T_G, rb = u / 28, cb = u - rb * 28;
      gemm_tile<2>(xn, DM, Wg, Wu, FF, DM, rb * 128, cb * 64, lds, rs, (unsigned)OFF_H, tid);
      signal_cnt(ws, C_H + rb);
    } else if (t < T_B) {
      wof_task(Wo, lds, rs, t - T_W, tid);
      signal_cnt(ws, C_WOF);
    } else if (t < T_S) {
      const int u = t - T_B, rb = u >> 2, cb = u & 3;
      if (threadIdx.x == 0) wait_cnt(ws, C_H + rb, 28);
      acquire_all();
      gemm_tile<3>((const bf16*)(ws + OFF_H), FF, Wd, nullptr, DM, FF, rb * 128, cb * 128, lds, rs, (unsigned)OFF_BOUT, tid);
      signal_cnt(ws, C_BO + rb);
    } else {
      const int u = t - T_S, seg = u / 20, k = u - seg * 20;
      if (k < 4) xattn_task(ws, rs, seg, k * 8 + __builtin_amdgcn_readfirstlane(tid >> 6), tid);
      else final_task(ws, rs, lds, x, out, seg, k - 4, tid);
    }
  }
}

constexpr int CL_SCR = 0;
constexpr int CL_KX = 0;
constexpr int CL_VX = CL_KX + 8192;
constexpr int CL_MEM = CL_VX + 4096;
constexpr int CL_QF = CL_MEM + 8192;
constexpr int CL_KF = CL_QF + 4096;
constexpr int CL_OF = CL_KF + 4096;
constexpr int CL_P = CL_OF + 2048;
constexpr int CL_WV = 32768;
constexpr int CL_WO = 98304;
constexpr int CHAIN_LDS = 163840;
#define STAMP(k)
constexpr unsigned SENT = 0xFFFFFFFFu;
constexpr unsigned SPIN_CAP = 1u << 22;

__device__ __forceinline__ void chain(const bf16* __restrict__ Wq, const bf16* __restrict__ Wk, const bf16* __restrict__ Wv,
                                      const bf16* __restrict__ Wo, char* ws, char* lds) {
  const int tid = threadIdx.x, lane = tid & 63, w = __builtin_amdgcn_readfirstlane(tid >> 6);
  const int li = lane & 15, g = lane >> 4, q4 = li >> 2, p4 = li & 3;
  const int b = blockIdx.x, h = b >> 1, c = b & 1;
  bf16x8 wqk[2][16];
  {
    constexpr int P1 = 272, P2 = 1040, P3 = 144;
    u32x4 r[8];
    auto ld_qk = [&](int it) {
#pragma unroll
      for (int u = 0; u < 8; ++u) {
        const int cidx = tid + 512 * u, mat = cidx >> 11, rr = (cidx >> 4) & 127, ch = cidx & 15;
        r[u] = *(const u32x4*)((mat ? Wk : Wq) + (size_t)(128 * it + rr) * (NH * DH) + h * DH + ch * 8);
      }
    };
    auto ld_wo = [&]() {
#pragma unroll
      for (int u = 0; u < 8; ++u) {
        const int cidx = tid + 512 * u, rr = cidx >> 6, ch = cidx & 63;
        r[u] = *(const u32x4*)(Wo + ((size_t)h * DH + c * 64 + rr) * DM + ch * 8);
      }
    };
    auto ld_wv = [&]() {
#pragma unroll
      for (int u = 0; u < 8; ++u) {
        const int rr = 64 * u + (tid >> 3), ch = tid & 7;
        r[u] = *(const u32x4*)(Wv + (size_t)rr * (NH * DH) + h * DH + c * 64 + ch * 8);
      }
    };
    char* scr = lds + 32768;
    ld_qk(0);
#pragma unroll
    for (int it = 0; it < 4; ++it) {
#pragma unroll
      for (int u = 0; u < 8; ++u) {
        const int cidx = tid + 512 * u, mat = cidx >> 11, rr = (cidx >> 4) & 127, ch = cidx & 15;
        *(u32x4*)(scr + mat * (128 * P1) + rr * P1 + ch * 16) = r[u];
      }
      __syncthreads();
      if (it < 3) ld_qk(it + 1); else ld_wo();
      const char* base = scr + (w >> 2) * (128 * P1);
#pragma unroll
      for (int k4 = 0; k4 < 4; ++k4)
#pragma unroll
        for (int t = 0; t < 2; ++t) {
          const int c16 = 2 * (w & 3) + t;
          bf16x4 lo = tr_read(base, (32 * k4 + 8 * g + q4) * P1 + (16 * c16 + 4 * p4) * 2);
          bf16x4 hi = tr_read(base, (32 * k4 + 8 * g + 4 + q4) * P1 + (16 * c16 + 4 * p4) * 2);
          wqk[t][4 * it + k4] = cat(lo, hi);
        }
      __syncthreads();
    }
    scr = lds;
#pragma unroll
    for (int u = 0; u < 8; ++u) {
      const int cidx = tid + 512 * u, rr = cidx >> 6, ch = cidx & 63;
      *(u32x4*)(scr + rr * P2 + ch * 16) = r[u];
    }
    __syncthreads();
    ld_wv();
#pragma unroll
    for (int nt = 0; nt < 4; ++nt)
#pragma unroll
      for (int k2 = 0; k2 < 2; ++k2) {
        const int col = 64 * w + 16 * nt + 4 * p4;
        bf16x4 lo = tr_read(scr, ((2 * k2 + 0) * 16 + 4 * g + q4) * P2 + col * 2);
        bf16x4 hi = tr_read(scr, ((2 * k2 + 1) * 16 + 4 * g + q4) * P2 + col * 2);
        *(bf16x8*)(lds + CL_WO + (((w * 4 + nt) * 2 + k2) * 64 + lane) * 16) = cat(lo, hi);
      }
    __syncthreads();
#pragma unroll
    for (int it = 0; it < 4; ++it) {
#pragma unroll
      for (int u = 0; u < 2; ++u) *(u32x4*)(scr + (64 * u + (tid >> 3)) * P3 + (tid & 7) * 16) = r[2 * it + u];
      __syncthreads();
#pragma unroll
      for (int e2 = 0; e2 < 2; ++e2) {
        const int et = 2 * (w & 1) + e2, k4 = w >> 1;
        bf16x4 lo = tr_read(scr, (32 * k4 + 8 * g + q4) * P3 + (16 * et + 4 * p4) * 2);
        bf16x4 hi = tr_read(scr, (32 * k4 + 8 * g + 4 + q4) * P3 + (16 * et + 4 * p4) * 2);
        *(bf16x8*)(lds + CL_WV + ((et * 16 + 4 * it + k4) * 64 + lane) * 16) = cat(lo, hi);
      }
      __syncthreads();
    }
  }
  const auto rs_mem = __builtin_amdgcn_make_buffer_rsrc(ws + OFF_MEMH, 0, 65 * 8192, 0x00020000);
  const auto rs_part = __builtin_amdgcn_make_buffer_rsrc(ws + OFF_PART, 0, 1 << 20, 0x00020000);
  const auto rs_km = __builtin_amdgcn_make_buffer_rsrc(ws + OFF_KMH, 0, 16 << 20, 0x00020000);
  const float scale = 0.08838834764831845f;
  const auto rs_kv = __builtin_amdgcn_make_buffer_rsrc(ws + OFF_KF, 0, 32 << 20, 0x00020000);
  unsigned rdy = 0;
  if (w < 4) {
    unsigned spins = 0;
    while (__builtin_amdgcn_readfirstlane(__hip_atomic_load(cnt_ptr(ws, C_KV), RLX_AGENT)) < 64u) { __builtin_amdgcn_s_sleep(4); if (++spins > SPIN_CAP) break; }
    const u32x4 a0 = __builtin_amdgcn_raw_buffer_load_b128(rs_kv, lane * 16, h * 8192 + w * 2048, 16);
    const u32x4 a1 = __builtin_amdgcn_raw_buffer_load_b128(rs_kv, lane * 16, h * 8192 + w * 2048 + 1024, 16);
    const u32x4 a2 = __builtin_amdgcn_raw_buffer_load_b128(rs_kv, lane * 16, (16 << 20) + h * 8192 + c * 4096 + w * 1024, 16);
    *(u32x4*)(lds + lane * 16 + CL_KX + w * 2048) = a0;
    *(u32x4*)(lds + lane * 16 + CL_KX + w * 2048 + 1024) = a1;
    *(u32x4*)(lds + lane * 16 + CL_VX + w * 1024) = a2;
    rdy = 64u;
  }
  const int pst_off = (((8 * w + 2 * (g & 1) + (g >> 1)) * 64 + b) * 8 + li) * 16;
  const char* l16 = lds + lane * 16;
  const char* mfb = lds + CL_MEM + (g * 8 + (li & 7)) * 16;
  const char* wvb = lds + CL_WV + ((w & 3) * 1024 + lane) * 16;
  const char* wob = lds + CL_WO + (w * 512 + lane) * 16;
  for (int s = 0; s < NSEG; ++s) {
    if (w >= 6) {
      u32x4 v[4]; unsigned spins = 0;
      asm volatile("s_waitcnt vmcnt(0)" ::: "memory");
      for (;;) {
        asm volatile("" ::: "memory");
#pragma unroll
        for (int u = 0; u < 4; ++u) v[u] = __builtin_amdgcn_raw_buffer_load_b128(rs_mem, lane * 16, s * 8192 + (w - 6) * 4096 + u * 1024, 16);
        unsigned m = 0;
#pragma unroll
        for (int u = 0; u < 4; ++u) m = max(m, max(v[u][0], v[u][2]));
        if (__all(m != SENT)) break;
        if (++spins > SPIN_CAP) break;
      }
#pragma unroll
      for (int u = 0; u < 4; ++u) *(u32x4*)(l16 + CL_MEM + (w - 6) * 4096 + u * 1024) = v[u];
    }
    STAMP(0);
    __syncthreads();
    STAMP(1);
    if (tid == 256 && s > 0) __hip_atomic_store(cnt_ptr(ws, C_CH + s - 1) + b, 1u, RLX_AGENT);
    u32x4 pf[3];
    const int sn = (s + 1 < NSEG) ? s + 1 : s;
    if (w < 4) {
      if (__builtin_amdgcn_readfirstlane(rdy) < 64u) {
        unsigned spins = 0;
        while (__builtin_amdgcn_readfirstlane(__hip_atomic_load(cnt_ptr(ws, C_KV + (sn >> 2)), RLX_AGENT)) < 64u) { __builtin_amdgcn_s_sleep(2); if (++spins > SPIN_CAP) break; }
      }
      const int so = (sn * NH + h) * 8192;
      pf[0] = __builtin_amdgcn_raw_buffer_load_b128(rs_kv, lane * 16, so + w * 2048, 16);
      pf[1] = __builtin_amdgcn_raw_buffer_load_b128(rs_kv, lane * 16, so + w * 2048 + 1024, 16);
      pf[2] = __builtin_amdgcn_raw_buffer_load_b128(rs_kv, lane * 16, (16 << 20) + so + c * 4096 + w * 1024, 16);
      const int s2 = (s + 2 < NSEG) ? s + 2 : NSEG - 1;
      rdy = __hip_atomic_load(cnt_ptr(ws, C_KV + (s2 >> 2)), RLX_AGENT);
    }
    bf16x8 mf[16];
#pragma unroll
    for (int ks = 0; ks < 16; ++ks) mf[ks] = *(const bf16x8*)(mfb + ks * 512);
    __builtin_amdgcn_sched_barrier(0);
    {
      f32x4 a0 = {0, 0, 0, 0}, a1 = {0, 0, 0, 0};
#pragma unroll
      for (int ks = 0; ks < 16; ++ks) {
        a0 = mfma32(wqk[0][ks], mf[ks], a0);
        a1 = mfma32(wqk[1][ks], mf[ks], a1);
      }
      const bf16x8 o = cat(pk4(a0), pk4(a1));
      *(bf16x8*)(lds + (w < 4 ? CL_QF : CL_KF) + ((w & 3) * 64 + lane) * 16) = o;
      if (w >= 4 && c == 0) __builtin_amdgcn_raw_buffer_store_b128(__builtin_bit_cast(u32x4, o), rs_km, lane * 16, ((s * NH + h) * 4 + (w & 3)) * 1024, 16);
    }
    STAMP(2);
    bf16x4 vm4 = {(__bf16)0.f, (__bf16)0.f, (__bf16)0.f, (__bf16)0.f};
    if (w == 0) {
      bf16x8 kxf[8];
#pragma unroll
      for (int u = 0; u < 8; ++u) kxf[u] = *(const bf16x8*)(l16 + CL_KX + u * 1024);
      __syncthreads();
      f32x4 s0 = {0, 0, 0, 0}, s1 = {0, 0, 0, 0}, s2 = {0, 0, 0, 0};
#pragma unroll
      for (int kk = 0; kk < 4; ++kk) {
        const bf16x8 qf = *(const bf16x8*)(l16 + CL_QF + kk * 1024);
        const bf16x8 k0 = *(const bf16x8*)(l16 + CL_KF + kk * 1024);
        s0 = mfma32(k0, qf, s0); s1 = mfma32(kxf[kk], qf, s1); s2 = mfma32(kxf[4 + kk], qf, s2);
      }
      if (g >= 2) s0 = (f32x4){-3.0e38f, -3.0e38f, -3.0e38f, -3.0e38f};
      float m = fmaxf(fmaxf(fmaxf(s0[0], s0[1]), fmaxf(s0[2], s0[3])), fmaxf(fmaxf(fmaxf(s1[0], s1[1]), fmaxf(s1[2], s1[3])), fmaxf(fmaxf(s2[0], s2[1]), fmaxf(s2[2], s2[3]))));
      m = xmax16_32(m);
      const float cexp = scale * 1.4426950408889634f, mc = -m * cexp;
      bf16x4 p0; bf16x8 p12;
#pragma unroll
      for (int r = 0; r < 4; ++r) {
        p0[r] = (__bf16)__builtin_amdgcn_exp2f(fmaf(s0[r], cexp, mc));
        p12[r] = (__bf16)__builtin_amdgcn_exp2f(fmaf(s1[r], cexp, mc));
        p12[4 + r] = (__bf16)__builtin_amdgcn_exp2f(fmaf(s2[r], cexp, mc));
      }
      *(bf16x8*)(lds + CL_P + lane * 16) = p12;
      *(bf16x4*)(lds + CL_P + 1024 + lane * 8) = p0;
    } else {
      if (w >= 4) {
        f32x4 va = {0, 0, 0, 0}, vb = {0, 0, 0, 0};
        bf16x8 wv[2][4];
#pragma unroll
        for (int k1 = 0; k1 < 4; ++k1) wv[0][k1] = *(const bf16x8*)(wvb + k1 * 1024);
        __syncthreads();
#pragma unroll
        for (int k4 = 0; k4 < 4; ++k4) {
          if (k4 < 3) {
#pragma unroll
            for (int k1 = 0; k1 < 4; ++k1) wv[(k4 + 1) & 1][k1] = *(const bf16x8*)(wvb + (4 * k4 + 4 + k1) * 1024);
          }
#pragma unroll
          for (int k1 = 0; k1 < 4; k1 += 2) { va = mfma32(mf[4 * k4 + k1], wv[k4 & 1][k1], va); vb = mfma32(mf[4 * k4 + k1 + 1], wv[k4 & 1][k1 + 1], vb); }
          __builtin_amdgcn_sched_barrier(0);
        }
        vm4 = pk4(va + vb);
        __builtin_amdgcn_raw_buffer_store_b64(__builtin_bit_cast(u32x2, vm4), rs_km, lane * 8, (8 << 20) + ((s * NH + h) * 8 + 4 * c + (w - 4)) * 512, 16);
      } else {
        __syncthreads();
      }
    }
    bf16x8 wo[4][2];
#pragma unroll
    for (int nt = 0; nt < 4; ++nt)
#pragma unroll
      for (int k2 = 0; k2 < 2; ++k2) wo[nt][k2] = *(const bf16x8*)(wob + (nt * 2 + k2) * 1024);
    __syncthreads();
    if (w >= 4) {
      const bf16x8 p12 = *(const bf16x8*)(lds + CL_P + lane * 16);
      const bf16x4 p0 = *(const bf16x4*)(lds + CL_P + 1024 + lane * 8);
      const bf16x8 vx8 = *(const bf16x8*)(lds + CL_VX + ((w - 4) * 64 + lane) * 16);
      f32x4 o = mfma16(vm4, p0, (f32x4){0, 0, 0, 0});
      const bf16x4 one4 = {(__bf16)1.f, (__bf16)1.f, (__bf16)1.f, (__bf16)1.f};
      f32x4 l = mfma16(one4, p0, (f32x4){0, 0, 0, 0});
      o = mfma32(vx8, p12, o);
      l = mfma32(cat(one4, one4), p12, l);
      const float inv = __builtin_amdgcn_rcpf(l[0]);
      *(bf16x4*)(lds + CL_OF + (((w - 4) >> 1) * 64 + lane) * 16 + (w & 1) * 8) = pk4(o * inv);
    }
    STAMP(3);
    __syncthreads();
    STAMP(4);
    if (s + 1 == NSEG) {
      if (w >= 4) asm volatile("s_waitcnt vmcnt(0)" ::: "memory");
      __syncthreads();
      if (tid == 64) __hip_atomic_store(cnt_ptr(ws, C_CH + s) + b, 1u, RLX_AGENT);
      break;
    }
    const int slot = (s & 1) << 19;
    {
      const bf16x8 of0 = *(const bf16x8*)(l16 + CL_OF);
      const bf16x8 of1 = *(const bf16x8*)(l16 + CL_OF + 1024);
#pragma unroll
      for (int n2 = 0; n2 < 2; ++n2) {
        f32x4 pa = mfma32(wo[2 * n2][0], of0, (f32x4){0, 0, 0, 0});
        f32x4 pb = mfma32(wo[2 * n2 + 1][0], of0, (f32x4){0, 0, 0, 0});
        pa = mfma32(wo[2 * n2][1], of1, pa);
        pb = mfma32(wo[2 * n2 + 1][1], of1, pb);
        u32x2 A = __builtin_bit_cast(u32x2, pk4(pa)), B = __builtin_bit_cast(u32x2, pk4(pb));
        auto r0 = __builtin_amdgcn_permlane16_swap(A[0], B[0], false, false);
        auto r1 = __builtin_amdgcn_permlane16_swap(A[1], B[1], false, false);
        u32x4 pv = {r0[0] | 1u, r1[0], r0[1] | 1u, r1[1]};
        if (li < 8) __builtin_amdgcn_raw_buffer_store_b128(pv, rs_part, pst_off, slot + n2 * 32768, 16);
      }
    }
    STAMP(5);
    if (w < 4) {
      *(u32x4*)(l16 + CL_KX + w * 2048) = pf[0];
      *(u32x4*)(l16 + CL_KX + w * 2048 + 1024) = pf[1];
      *(u32x4*)(l16 + CL_VX + w * 1024) = pf[2];
    }
    if (w == 4 || w == 5) asm volatile("s_waitcnt vmcnt(0)" ::: "memory");
    if (w >= 6) {
      u32x2 v[8]; unsigned spins = 0;
      const int roff = lane * 16 + (w - 6) * 8;
      asm volatile("s_waitcnt vmcnt(0)" ::: "memory");
      for (;;) {
        asm volatile("" ::: "memory");
#pragma unroll
        for (int it = 0; it < 8; ++it) v[it] = __builtin_amdgcn_raw_buffer_load_b64(rs_part, roff, slot + b * 8192 + it * 1024, 16);
        unsigned m = 1u;
#pragma unroll
        for (int it = 0; it < 8; ++it) m &= v[it][0];
        if (__all(m != 0u)) break;
        if (++spins > SPIN_CAP) break;
      }
      STAMP(6);
      f32x4 a = {0, 0, 0, 0};
#pragma unroll
      for (int it = 0; it < 8; ++it) {
        a[0] += __builtin_bit_cast(float, v[it][0] << 16); a[1] += __builtin_bit_cast(float, v[it][0] & 0xffff0000u);
        a[2] += __builtin_bit_cast(float, v[it][1] << 16); a[3] += __builtin_bit_cast(float, v[it][1] & 0xffff0000u);
      }
#pragma unroll
      for (int r = 0; r < 4; ++r) a[r] = xsum8_16_32(a[r]);
      u32x2 o = __builtin_bit_cast(u32x2, pk4(a));
      if (o[0] == SENT) o[0] = 0x7FFF7FFFu;
      if (lane < 8) __builtin_amdgcn_raw_buffer_store_b64(o, rs_mem, lane * 16 + (w - 6) * 8, (s + 1) * 8192 + b * 128, 16);
      const u32x2 z = {0u, 0u};
#pragma unroll
      for (int it = 0; it < 8; ++it) __builtin_amdgcn_raw_buffer_store_b64(z, rs_part, roff, slot + b * 8192 + it * 1024, 16);
      STAMP(7);
    }
  }
}

__global__ __launch_bounds__(512, 2) void k_main(const bf16* __restrict__ x, const bf16* __restrict__ Wq, const bf16* __restrict__ Wk,
                                                 const bf16* __restrict__ Wv, const bf16* __restrict__ Wo, const bf16* __restrict__ Wg,
                                                 const bf16* __restrict__ Wu, const bf16* __restrict__ Wd, bf16* __restrict__ out, char* ws) {
  extern __shared__ __attribute__((aligned(16))) char lds[];
  if (blockIdx.x < 64) chain(Wq, Wk, Wv, Wo, ws, lds);
  else worker(x, Wq, Wk, Wv, Wo, Wg, Wu, Wd, out, ws, lds);
}

extern "C" void kernel_launch(void* const* d_in, const int* in_sizes, int n_in,
                              void* d_out, int out_size, void* d_ws, size_t ws_size,
                              hipStream_t stream) {
  const bf16* x = (const bf16*)d_in[0];
  const bf16* mem0 = (const bf16*)d_in[1];
  const bf16* Wq = (const bf16*)d_in[2];
  const bf16* Wk = (const bf16*)d_in[3];
  const bf16* Wv = (const bf16*)d_in[4];
  const bf16* Wo = (const bf16*)d_in[5];
  const bf16* Wg = (const bf16*)d_in[8];
  const bf16* Wu = (const bf16*)d_in[9];
  const bf16* Wd = (const bf16*)d_in[10];
  char* ws = (char*)d_ws;
  (void)hipFuncSetAttribute((const void*)k_main, hipFuncAttributeMaxDynamicSharedMemorySize, CHAIN_LDS);
  k_ln<<<S_ / 4, 256, 0, stream>>>(x, mem0, ws);
  k_main<<<256, 512, CHAIN_LDS, stream>>>(x, Wq, Wk, Wv, Wo, Wg, Wu, Wd, (bf16*)d_out, ws);
}
```

```cpp
#include <hip/hip_runtime.h>
#include <hip/hip_bf16.h>

constexpr int S_ = 2048, DM = 512, NH = 32, DH = 128, MM = 8, SEG = 32, NSEG = 64, FF = 1792;

typedef __bf16 bf16;
typedef __bf16 bf16x8 __attribute__((ext_vector_type(8)));
typedef __bf16 bf16x4 __attribute__((ext_vector_type(4)));
typedef short s16x4 __attribute__((ext_vector_type(4)));
typedef float f32x4 __attribute__((ext_vector_type(4)));
typedef unsigned u32x4 __attribute__((ext_vector_type(4)));
typedef unsigned u32x2 __attribute__((ext_vector_type(2)));
#define LAS __attribute__((address_space(3)))

constexpr size_t OFF_MEMH = 0;
constexpr size_t OFF_PART = 1 << 20;
constexpr size_t OFF_CNT  = 2 << 20;
constexpr size_t OFF_XN   = 4u << 20;
constexpr size_t OFF_BOUT = 6u << 20;
constexpr size_t OFF_H    = 8u << 20;
constexpr size_t OFF_QF   = 16u << 20;
constexpr size_t OFF_KF   = 32u << 20;
constexpr size_t OFF_VF   = 48u << 20;
constexpr size_t OFF_OX   = 64u << 20;
constexpr size_t OFF_KMH  = 80u << 20;
constexpr size_t OFF_VMH  = 88u << 20;
constexpr size_t OFF_WOF  = 96u << 20;
constexpr unsigned WS_SPAN = 100u << 20;
constexpr int C_TICKET = 0, C_KV = 1, C_Q = 17, C_H = 33, C_BO = 49, C_WOF = 65, C_X = 66, C_CH = 130;
#define RLX_AGENT __ATOMIC_RELAXED, __HIP_MEMORY_SCOPE_AGENT

__device__ __forceinline__ f32x4 mfma32(bf16x8 a, bf16x8 b, f32x4 c) {
  return __builtin_amdgcn_mfma_f32_16x16x32_bf16(a, b, c, 0, 0, 0);
}
__device__ __forceinline__ f32x4 mfma16(bf16x4 a, bf16x4 b, f32x4 c) {
  bf16x8 a8, b8;
#pragma unroll
  for (int j = 0; j < 4; ++j) { a8[j] = a[j]; b8[j] = b[j]; a8[4 + j] = (__bf16)0.f; b8[4 + j] = (__bf16)0.f; }
  return __builtin_amdgcn_mfma_f32_16x16x32_bf16(a8, b8, c, 0, 0, 0);
}
__device__ __forceinline__ float bfr(float x) { return (float)(__bf16)x; }
__device__ __forceinline__ bf16x4 tr_read(const char* lds_base, unsigned off) {
  s16x4 t = __builtin_amdgcn_ds_read_tr16_b64_v4i16((LAS s16x4*)(lds_base + off));
  return __builtin_bit_cast(bf16x4, t);
}
__device__ __forceinline__ bf16x8 cat(bf16x4 a, bf16x4 b) {
  bf16x8 r; r[0] = a[0]; r[1] = a[1]; r[2] = a[2]; r[3] = a[3]; r[4] = b[0]; r[5] = b[1]; r[6] = b[2]; r[7] = b[3]; return r;
}
__device__ __forceinline__ bf16x4 pk4(f32x4 v) { bf16x4 r; r[0] = (__bf16)v[0]; r[1] = (__bf16)v[1]; r[2] = (__bf16)v[2]; r[3] = (__bf16)v[3]; return r; }

__device__ __forceinline__ float xmax16_32(float x) {
  unsigned u = __builtin_bit_cast(unsigned, x);
  auto a = __builtin_amdgcn_permlane16_swap(u, u, false, false);
  x = fmaxf(__builtin_bit_cast(float, (unsigned)a[0]), __builtin_bit_cast(float, (unsigned)a[1]));
  u = __builtin_bit_cast(unsigned, x);
  auto c = __builtin_amdgcn_permlane32_swap(u, u, false, false);
  return fmaxf(__builtin_bit_cast(float, (unsigned)c[0]), __builtin_bit_cast(float, (unsigned)c[1]));
}
__device__ __forceinline__ float xsum8_16_32(float x) {
  x += __builtin_bit_cast(float, __builtin_amdgcn_update_dpp(0, __builtin_bit_cast(int, x), 0x128, 0xf, 0xf, false));
  unsigned u = __builtin_bit_cast(unsigned, x);
  auto a = __builtin_amdgcn_permlane16_swap(u, u, false, false);
  x = __builtin_bit_cast(float, (unsigned)a[0]) + __builtin_bit_cast(float, (unsigned)a[1]);
  u = __builtin_bit_cast(unsigned, x);
  auto c = __builtin_amdgcn_permlane32_swap(u, u, false, false);
  return __builtin_bit_cast(float, (unsigned)c[0]) + __builtin_bit_cast(float, (unsigned)c[1]);
}

__global__ __launch_bounds__(256) void k_ln(const bf16* __restrict__ x, const bf16* __restrict__ mem0, char* ws) {
  const int lane = threadIdx.x & 63, row = blockIdx.x * 4 + (threadIdx.x >> 6);
  bf16* xn = (bf16*)(ws + OFF_XN);
  bf16x8 v = *(const bf16x8*)(x + (size_t)row * DM + lane * 8);
  float f[8], s = 0.f;
#pragma unroll
  for (int j = 0; j < 8; ++j) { f[j] = (float)v[j]; s += f[j]; }
#pragma unroll
  for (int m = 32; m >= 1; m >>= 1) s += __shfl_xor(s, m);
  const float mean = s * (1.f / DM);
  float q = 0.f;
#pragma unroll
  for (int j = 0; j < 8; ++j) { float d = f[j] - mean; q += d * d; }
#pragma unroll
  for (int m = 32; m >= 1; m >>= 1) q += __shfl_xor(q, m);
  const float mb = bfr(mean), vb = bfr(q * (1.f / DM));
  const float den = bfr(sqrtf(bfr(vb + 1e-5f)));
  bf16x8 o;
#pragma unroll
  for (int j = 0; j < 8; ++j) o[j] = (__bf16)(bfr(f[j] - mb) / den);
  *(bf16x8*)(xn + (size_t)row * DM + lane * 8) = o;
  {
    const u32x4 ff = {0xFFFFFFFFu, 0xFFFFFFFFu, 0xFFFFFFFFu, 0xFFFFFFFFu}, zz = {0u, 0u, 0u, 0u};
    if (threadIdx.x < 64) *(u32x4*)(ws + OFF_MEMH + 8192 + ((size_t)blockIdx.x * 64 + threadIdx.x) * 16) = ff;
    if (threadIdx.x < 136) *(u32x4*)(ws + OFF_PART + ((size_t)blockIdx.x * 136 + threadIdx.x) * 16) = zz;
  }
  if (blockIdx.x == 0) {
    for (int c = threadIdx.x; c < 512; c += 256) {
      const int i = c & 7, dg = c >> 3;
      *(bf16x8*)(ws + OFF_MEMH + (size_t)c * 16) = *(const bf16x8*)(mem0 + i * DM + dg * 8);
    }
  }
}

constexpr int GA_PITCH = 272;
constexpr int GA_BYTES = 128 * GA_PITCH;
constexpr int GB_BYTES = 128 * 256;
__device__ __forceinline__ unsigned off_b(unsigned row, unsigned ch) { return 256u * row + 16u * (ch ^ (((row & 3) << 2) | ((row >> 2) & 3))); }
typedef __amdgpu_buffer_rsrc_t rsrc_t;
__device__ __forceinline__ void st16(rsrc_t rs, unsigned off, bf16x8 v) { __builtin_amdgcn_raw_buffer_store_b128(__builtin_bit_cast(u32x4, v), rs, off, 0, 16); }
__device__ __forceinline__ void st8(rsrc_t rs, unsigned off, bf16x4 v) { __builtin_amdgcn_raw_buffer_store_b64(__builtin_bit_cast(u32x2, v), rs, off, 0, 16); }

__device__ __forceinline__ bf16x8 ld16(rsrc_t rs, unsigned voff, unsigned soff) { return __builtin_bit_cast(bf16x8, __builtin_amdgcn_raw_buffer_load_b128(rs, voff, soff, 16)); }
__device__ __forceinline__ bf16x4 ld8(rsrc_t rs, unsigned voff, unsigned soff) { return __builtin_bit_cast(bf16x4, __builtin_amdgcn_raw_buffer_load_b64(rs, voff, soff, 16)); }

template <int MODE>
__device__ __forceinline__ void gemm_tile(const bf16* __restrict__ A, int lda, const bf16* __restrict__ B0, const bf16* __restrict__ B1,
                                          int ldb, int K, int row0, int col0, char* lds, rsrc_t rs, unsigned dst, int tid) {
  const int lane = tid & 63, w = tid >> 6, wm = w >> 1, wn = w & 1;
  const int li = lane & 15, g = lane >> 4, q = li >> 2, p = li & 3;
  char* la = lds; char* lb = lds + GA_BYTES;
  f32x4 acc[2][4];
#pragma unroll
  for (int a = 0; a < 2; ++a)
#pragma unroll
    for (int b = 0; b < 4; ++b) acc[a][b] = (f32x4){0.f, 0.f, 0.f, 0.f};
  const bf16* asrc[4]; const bf16* bsrc[4]; unsigned adst[4], bdst[4];
#pragma unroll
  for (int u = 0; u < 4; ++u) {
    const int c = tid + 512 * u;
    asrc[u] = A + (size_t)(row0 + (c >> 4)) * lda + (c & 15) * 8;
    adst[u] = (c >> 4) * GA_PITCH + (c & 15) * 16;
    const int kr = c >> 4, ch = c & 15;
    if (MODE == 2) {
      const int blk = ch >> 2;
      bsrc[u] = ((blk & 1) ? B1 : B0) + (size_t)kr * ldb + col0 + (blk >> 1) * 32 + (ch & 3) * 8;
    } else {
      bsrc[u] = B0 + (size_t)kr * ldb + col0 + ch * 8;
    }
    bdst[u] = off_b(kr, ch);
  }
  auto compute = [&]() {
#pragma unroll
    for (int ks = 0; ks < 4; ++ks) {
      bf16x8 af[2], bfr_[4];
#pragma unroll
      for (int tt = 0; tt < 2; ++tt) af[tt] = *(const bf16x8*)(la + (wm * 32 + tt * 16 + li) * GA_PITCH + ks * 64 + g * 16);
#pragma unroll
      for (int nt = 0; nt < 4; ++nt) {
        const unsigned ch = 2 * (wn * 4 + nt) + (p >> 1);
        bf16x4 lo = tr_read(lb, off_b(32 * ks + 8 * g + q, ch) + 8 * (p & 1));
        bf16x4 hi = tr_read(lb, off_b(32 * ks + 8 * g + 4 + q, ch) + 8 * (p & 1));
        bfr_[nt] = cat(lo, hi);
      }
#pragma unroll
      for (int tt = 0; tt < 2; ++tt)
#pragma unroll
        for (int nt = 0; nt < 4; ++nt) {
          if (MODE == 1) acc[tt][nt] = mfma32(af[tt], bfr_[nt], acc[tt][nt]);
          else acc[tt][nt] = mfma32(bfr_[nt], af[tt], acc[tt][nt]);
        }
    }
  };
  if (false) {
    u32x4 ra[4][4], rb[4][4];
#pragma unroll
    for (int kt = 0; kt < 4; ++kt)
#pragma unroll
      for (int u = 0; u < 4; ++u) { ra[kt][u] = *(const u32x4*)(asrc[u] + kt * 128); rb[kt][u] = *(const u32x4*)(bsrc[u] + (size_t)kt * 128 * ldb); }
#pragma unroll
    for (int kt = 0; kt < 4; ++kt) {
#pragma unroll
      for (int u = 0; u < 4; ++u) { *(u32x4*)(la + adst[u]) = ra[kt][u]; *(u32x4*)(lb + bdst[u]) = rb[kt][u]; }
      __syncthreads();
      compute();
      __syncthreads();
    }
  } else {
    u32x4 ra[4], rb[4];
#pragma unroll
    for (int u = 0; u < 4; ++u) { ra[u] = *(const u32x4*)asrc[u]; rb[u] = *(const u32x4*)bsrc[u]; }
    const int nkt = K >> 7;
    for (int kt = 0; kt < nkt; ++kt) {
#pragma unroll
      for (int u = 0; u < 4; ++u) { *(u32x4*)(la + adst[u]) = ra[u]; *(u32x4*)(lb + bdst[u]) = rb[u]; }
      __syncthreads();
      if (kt + 1 < nkt) {
#pragma unroll
        for (int u = 0; u < 4; ++u) {
          ra[u] = *(const u32x4*)(asrc[u] + (kt + 1) * 128);
          rb[u] = *(const u32x4*)(bsrc[u] + (size_t)(kt + 1) * 128 * ldb);
        }
      }
      compute();
      __syncthreads();
    }
  }
  if (MODE == 0) {
    const int seg = (row0 >> 5) + wm, head = col0 >> 7;
#pragma unroll
    for (int tt = 0; tt < 2; ++tt)
#pragma unroll
      for (int k2 = 0; k2 < 2; ++k2)
        st16(rs, dst + ((((seg * NH + head) * 2 + tt) * 4 + (wn * 2 + k2)) * 64 + lane) * 16, cat(pk4(acc[tt][2 * k2]), pk4(acc[tt][2 * k2 + 1])));
  } else if (MODE == 1) {
    const int seg = (row0 >> 5) + wm, head = col0 >> 7;
#pragma unroll
    for (int nt = 0; nt < 4; ++nt)
      st16(rs, dst + (((seg * NH + head) * 8 + (wn * 4 + nt)) * 64 + lane) * 16, cat(pk4(acc[0][nt]), pk4(acc[1][nt])));
  } else if (MODE == 2) {
#pragma unroll
    for (int tt = 0; tt < 2; ++tt)
#pragma unroll
      for (int n2 = 0; n2 < 2; ++n2) {
        f32x4 hv;
#pragma unroll
        for (int r = 0; r < 4; ++r) {
          const float gg = bfr(acc[tt][n2][r]), uu = bfr(acc[tt][n2 + 2][r]);
          const float sg = bfr(1.f / (1.f + __expf(-gg)));
          hv[r] = sg * uu;
        }
        const int row = row0 + wm * 32 + tt * 16 + li, col = col0 + 32 * wn + 16 * n2 + 4 * g;
        st8(rs, dst + (row * FF + col) * 2, pk4(hv));
      }
  } else {
#pragma unroll
    for (int tt = 0; tt < 2; ++tt)
#pragma unroll
      for (int nt = 0; nt < 4; ++nt) {
        const int row = row0 + wm * 32 + tt * 16 + li, col = col0 + 64 * wn + 16 * nt + 4 * g;
        st8(rs, dst + (row * DM + col) * 2, pk4(acc[tt][nt]));
      }
  }
}

__device__ __forceinline__ unsigned* cnt_ptr(char* ws, int idx) { return (unsigned*)(ws + OFF_CNT) + idx * 64; }
__device__ __forceinline__ void wait_cnt(char* ws, int idx, unsigned target) {
  unsigned spins = 0;
  while (__hip_atomic_load(cnt_ptr(ws, idx), RLX_AGENT) < target) { __builtin_amdgcn_s_sleep(4); if (++spins > (1u << 18)) break; }
}
__device__ __forceinline__ void acquire_all() {
  if (threadIdx.x == 0) { __builtin_amdgcn_fence(__ATOMIC_ACQUIRE, "agent"); asm volatile("s_waitcnt vmcnt(0)" ::: "memory"); }
  __syncthreads();
}
__device__ __forceinline__ void signal_cnt(char* ws, int idx) {
  asm volatile("s_waitcnt vmcnt(0)" ::: "memory");
  __syncthreads();
  if (threadIdx.x == 0) __hip_atomic_fetch_add(cnt_ptr(ws, idx), 1u, RLX_AGENT);
}

__device__ __forceinline__ void xattn_task(char* ws, rsrc_t rs, int seg, int head, int tid) {
  const int lane = tid & 63, li = lane & 15, g = lane >> 4;
  const int unit = seg * NH + head, rb = seg >> 2;
  const unsigned qf = (unsigned)OFF_QF + unit * 8192, kf = (unsigned)OFF_KF + unit * 8192, vf = (unsigned)OFF_VF + unit * 8192;
  const unsigned km = (unsigned)OFF_KMH + unit * 4096, vm = (unsigned)OFF_VMH + unit * 4096;
  const float scale = 0.08838834764831845f;
  if (threadIdx.x == 0) { wait_cnt(ws, C_Q + rb, 32); wait_cnt(ws, C_KV + rb, 64); }
  __syncthreads();
  f32x4 sc[3][2];
#pragma unroll
  for (int a = 0; a < 3; ++a) { sc[a][0] = (f32x4){0, 0, 0, 0}; sc[a][1] = (f32x4){0, 0, 0, 0}; }
  bf16x8 q[2][4], vx[8];
#pragma unroll
  for (int kk = 0; kk < 4; ++kk) {
    q[0][kk] = ld16(rs, lane * 16, qf + kk * 1024); q[1][kk] = ld16(rs, lane * 16, qf + 4096 + kk * 1024);
    const bf16x8 k1 = ld16(rs, lane * 16, kf + kk * 1024), k2 = ld16(rs, lane * 16, kf + 4096 + kk * 1024);
    sc[1][0] = mfma32(k1, q[0][kk], sc[1][0]); sc[1][1] = mfma32(k1, q[1][kk], sc[1][1]);
    sc[2][0] = mfma32(k2, q[0][kk], sc[2][0]); sc[2][1] = mfma32(k2, q[1][kk], sc[2][1]);
  }
#pragma unroll
  for (int et = 0; et < 8; ++et) vx[et] = ld16(rs, lane * 16, vf + et * 1024);
  if (threadIdx.x < 64) {
    unsigned spins = 0;
    while (!__all(__hip_atomic_load(cnt_ptr(ws, C_CH + seg) + threadIdx.x, RLX_AGENT) != 0u)) { __builtin_amdgcn_s_sleep(4); if (++spins > (1u << 18)) break; }
  }
  __syncthreads();
  bf16x8 k0[4]; bf16x4 vmf[8];
#pragma unroll
  for (int kk = 0; kk < 4; ++kk) k0[kk] = ld16(rs, lane * 16, km + kk * 1024);
#pragma unroll
  for (int et = 0; et < 8; ++et) vmf[et] = ld8(rs, lane * 8, vm + et * 512);
#pragma unroll
  for (int kk = 0; kk < 4; ++kk) { sc[0][0] = mfma32(k0[kk], q[0][kk], sc[0][0]); sc[0][1] = mfma32(k0[kk], q[1][kk], sc[0][1]); }
  bf16x4 p0[2]; bf16x8 p12[2];
#pragma unroll
  for (int qt = 0; qt < 2; ++qt) {
    const int qi = 16 * qt + li;
    float v[12]; float m = -3.0e38f;
#pragma unroll
    for (int kt = 0; kt < 3; ++kt)
#pragma unroll
      for (int r = 0; r < 4; ++r) {
        float s = bfr(bfr(sc[kt][qt][r]) * scale);
        bool ok = (kt == 0) ? (g < 2) : (16 * (kt - 1) + 4 * g + r <= qi);
        s = ok ? s : -3.0e38f;
        v[kt * 4 + r] = s; m = fmaxf(m, s);
      }
    m = xmax16_32(m);
    float sum = 0.f;
#pragma unroll
    for (int e = 0; e < 12; ++e) { v[e] = (v[e] > -1.0e38f) ? __expf(v[e] - m) : 0.f; sum += v[e]; }
    sum += __shfl_xor(sum, 16); sum += __shfl_xor(sum, 32);
    const float inv = 1.f / sum;
#pragma unroll
    for (int r = 0; r < 4; ++r) { p0[qt][r] = (__bf16)(v[r] * inv); p12[qt][r] = (__bf16)(v[4 + r] * inv); p12[qt][4 + r] = (__bf16)(v[8 + r] * inv); }
  }
#pragma unroll
  for (int kk = 0; kk < 4; ++kk) {
    f32x4 o[2][2];
#pragma unroll
    for (int e2 = 0; e2 < 2; ++e2)
#pragma unroll
      for (int qt = 0; qt < 2; ++qt) {
        o[qt][e2] = mfma16(vmf[2 * kk + e2], p0[qt], (f32x4){0, 0, 0, 0});
        o[qt][e2] = mfma32(vx[2 * kk + e2], p12[qt], o[qt][e2]);
      }
#pragma unroll
    for (int qt = 0; qt < 2; ++qt)
      st16(rs, (unsigned)OFF_OX + ((((seg * 2 + qt) * NH + head) * 4 + kk) * 64 + lane) * 16, cat(pk4(o[qt][0]), pk4(o[qt][1])));
  }
  signal_cnt(ws, C_X + seg);
}

__device__ __forceinline__ void wof_task(const bf16* __restrict__ Wo, char* lds, rsrc_t rs, int t, int tid) {
  const int lane = tid & 63, w = tid >> 6, li = lane & 15, g = lane >> 4, q4 = li >> 2, p4 = li & 3;
  const int h = t >> 1, chh = t & 1;
  constexpr int P = 528;
#pragma unroll
  for (int u = 0; u < 8; ++u) {
    const int cidx = tid + 512 * u, rr = cidx >> 5, ch = cidx & 31;
    *(u32x4*)(lds + rr * P + ch * 16) = *(const u32x4*)(Wo + ((size_t)h * DH + rr) * DM + chh * 256 + ch * 8);
  }
  __syncthreads();
#pragma unroll
  for (int n2 = 0; n2 < 2; ++n2)
#pragma unroll
    for (int kk = 0; kk < 4; ++kk) {
      const int ntl = 2 * w + n2;
      bf16x4 lo = tr_read(lds, ((2 * kk + 0) * 16 + 4 * g + q4) * P + (16 * ntl + 4 * p4) * 2);
      bf16x4 hi = tr_read(lds, ((2 * kk + 1) * 16 + 4 * g + q4) * P + (16 * ntl + 4 * p4) * 2);
      st16(rs, (unsigned)OFF_WOF + ((((chh * 16 + ntl) * NH + h) * 4 + kk) * 64 + lane) * 16, cat(lo, hi));
    }
}

__device__ __forceinline__ void final_task(char* ws, rsrc_t rs, char* lds, const bf16* __restrict__ x, bf16* __restrict__ out, int seg, int cb, int tid) {
  const int lane = tid & 63, w = __builtin_amdgcn_readfirstlane(tid >> 6);
  if (tid == 0) { wait_cnt(ws, C_WOF, 64); wait_cnt(ws, C_BO + (seg >> 2), 4); }
  __syncthreads();
  bf16x8 bf[16][2];
#pragma unroll
  for (int i = 0; i < 16; ++i)
#pragma unroll
    for (int nt = 0; nt < 2; ++nt) bf[i][nt] = ld16(rs, lane * 16, (unsigned)OFF_WOF + ((cb * 2 + nt) * 128 + w * 16 + i) * 1024);
  const int tt_ = (tid >> 7) & 1, nt_ = (tid >> 6) & 1, li = lane & 15, g = lane >> 4;
  const int row = seg * 32 + tt_ * 16 + li, col = cb * 32 + nt_ * 16 + 4 * g;
  const bf16x4 xv = *(const bf16x4*)(x + (size_t)row * DM + col);
  const bf16x4 bv = ld8(rs, (row * DM + col) * 2, (unsigned)OFF_BOUT);
  if (tid == 0) wait_cnt(ws, C_X + seg, 4);
  __syncthreads();
  f32x4 acc[2][2];
#pragma unroll
  for (int a = 0; a < 2; ++a) { acc[a][0] = (f32x4){0.f, 0.f, 0.f, 0.f}; acc[a][1] = (f32x4){0.f, 0.f, 0.f, 0.f}; }
#pragma unroll
  for (int i4 = 0; i4 < 4; ++i4) {
    bf16x8 af[4][2];
#pragma unroll
    for (int i = 0; i < 4; ++i)
#pragma unroll
      for (int tt = 0; tt < 2; ++tt) af[i][tt] = ld16(rs, lane * 16, (unsigned)OFF_OX + ((seg * 2 + tt) * 128 + w * 16 + i4 * 4 + i) * 1024);
#pragma unroll
    for (int i = 0; i < 4; ++i)
#pragma unroll
      for (int tt = 0; tt < 2; ++tt)
#pragma unroll
        for (int nt = 0; nt < 2; ++nt) acc[tt][nt] = mfma32(bf[i4 * 4 + i][nt], af[i][tt], acc[tt][nt]);
    if (i4 == 1) __builtin_amdgcn_sched_barrier(0);
  }
#pragma unroll
  for (int tt = 0; tt < 2; ++tt)
#pragma unroll
    for (int nt = 0; nt < 2; ++nt) *(f32x4*)(lds + (((w * 2 + tt) * 2 + nt) * 64 + lane) * 16) = acc[tt][nt];
  __syncthreads();
  if (tid < 256) {
    f32x4 a = {0, 0, 0, 0};
#pragma unroll
    for (int ww = 0; ww < 8; ++ww) a += *(const f32x4*)(lds + (((ww * 2 + tt_) * 2 + nt_) * 64 + lane) * 16);
    f32x4 o;
#pragma unroll
    for (int r = 0; r < 4; ++r) o[r] = bfr((float)xv[r] + bfr(a[r])) + (float)bv[r];
    *(bf16x4*)(out + (size_t)row * DM + col) = pk4(o);
  }
}

constexpr int T_KV = 0, T_Q = T_KV + 1024, T_G = T_Q + 512, T_W = T_G + 448, T_B = T_W + 64, T_S = T_B + 64, T_END = T_S + 64 * 20;

__device__ __forceinline__ void worker(const bf16* __restrict__ x, const bf16* __restrict__ Wq, const bf16* __restrict__ Wk, const bf16* __restrict__ Wv,
                                       const bf16* __restrict__ Wo, const bf16* __restrict__ Wg, const bf16* __restrict__ Wu, const bf16* __restrict__ Wd,
                                       bf16* __restrict__ out, char* ws, char* lds) {
  const rsrc_t rs = __builtin_amdgcn_make_buffer_rsrc(ws, 0, WS_SPAN, 0x00020000);
  const bf16* xn = (const bf16*)(ws + OFF_XN);
  volatile int* tk = (volatile int*)(lds + 144 * 1024);
  for (;;) {
    __syncthreads();
    if (threadIdx.x == 0) *tk = (int)__hip_atomic_fetch_add(cnt_ptr(ws, C_TICKET), 1u, RLX_AGENT);
    __syncthreads();
    const int t = *tk;
    int tid = threadIdx.x; asm volatile("" : "+v"(tid));
    if (t >= T_END) break;
    if (t < T_Q) {
      const int rb = t >> 6, cb = t & 63;
      if (cb < 32) gemm_tile<0>(xn, DM, Wk, nullptr, NH * DH, DM, rb * 128, cb * 128, lds, rs, (unsigned)OFF_KF, tid);
      else gemm_tile<1>(xn, DM, Wv, nullptr, NH * DH, DM, rb * 128, (cb - 32) * 128, lds, rs, (unsigned)OFF_VF, tid);
      signal_cnt(ws, C_KV + rb);
    } else if (t < T_G) {
      const int u = t - T_Q, rb = u >> 5, cb = u & 31;
      gemm_tile<0>(xn, DM, Wq, nullptr, NH * DH, DM, rb * 128, cb * 128, lds, rs, (unsigned)OFF_QF, tid);
      signal_cnt(ws, C_Q + rb);
    } else if (t < T_W) {
      const int u = t - T_G, rb = u / 28, cb = u - rb * 28;
      gemm_tile<2>(xn, DM, Wg, Wu, FF, DM, rb * 128, cb * 64, lds, rs, (unsigned)OFF_H, tid);
      signal_cnt(ws, C_H + rb);
    } else if (t < T_B) {
      wof_task(Wo, lds, rs, t - T_W, tid);
      signal_cnt(ws, C_WOF);
    } else if (t < T_S) {
      const int u = t - T_B, rb = u >> 2, cb = u & 3;
      if (threadIdx.x == 0) wait_cnt(ws, C_H + rb, 28);
      acquire_all();
      gemm_tile<3>((const bf16*)(ws + OFF_H), FF, Wd, nullptr, DM, FF, rb * 128, cb * 128, lds, rs, (unsigned)OFF_BOUT, tid);
      signal_cnt(ws, C_BO + rb);
    } else {
      const int u = t - T_S, seg = u / 20, k = u - seg * 20;
      if (k < 4) xattn_task(ws, rs, seg, k * 8 + __builtin_amdgcn_readfirstlane(tid >> 6), tid);
      else final_task(ws, rs, lds, x, out, seg, k - 4, tid);
    }
  }
}

constexpr int CL_SCR = 0;
constexpr int CL_KX = 0;
constexpr int CL_VX = CL_KX + 8192;
constexpr int CL_MEM = CL_VX + 4096;
constexpr int CL_QF = CL_MEM + 8192;
constexpr int CL_KF = CL_QF + 4096;
constexpr int CL_OF = CL_KF + 4096;
constexpr int CL_P = CL_OF + 2048;
constexpr int CL_WV = 32768;
constexpr int CL_WO = 98304;
constexpr int CHAIN_LDS = 163840;
#define STAMP(k)
constexpr unsigned SENT = 0xFFFFFFFFu;
constexpr unsigned SPIN_CAP = 1u << 18;

__device__ __forceinline__ void chain(const bf16* __restrict__ Wq, const bf16* __restrict__ Wk, const bf16* __restrict__ Wv,
                                      const bf16* __restrict__ Wo, char* ws, char* lds) {
  const int tid = threadIdx.x, lane = tid & 63, w = __builtin_amdgcn_readfirstlane(tid >> 6);
  const int li = lane & 15, g = lane >> 4, q4 = li >> 2, p4 = li & 3;
  const int b = blockIdx.x, h = b >> 1, c = b & 1;
  bf16x8 wqk[2][16];
  {
    constexpr int P1 = 272, P2 = 1040, P3 = 144;
    u32x4 r[8];
    auto ld_qk = [&](int it) {
#pragma unroll
      for (int u = 0; u < 8; ++u) {
        const int cidx = tid + 512 * u, mat = cidx >> 11, rr = (cidx >> 4) & 127, ch = cidx & 15;
        r[u] = *(const u32x4*)((mat ? Wk : Wq) + (size_t)(128 * it + rr) * (NH * DH) + h * DH + ch * 8);
      }
    };
    auto ld_wo = [&]() {
#pragma unroll
      for (int u = 0; u < 8; ++u) {
        const int cidx = tid + 512 * u, rr = cidx >> 6, ch = cidx & 63;
        r[u] = *(const u32x4*)(Wo + ((size_t)h * DH + c * 64 + rr) * DM + ch * 8);
      }
    };
    auto ld_wv = [&]() {
#pragma unroll
      for (int u = 0; u < 8; ++u) {
        const int rr = 64 * u + (tid >> 3), ch = tid & 7;
        r[u] = *(const u32x4*)(Wv + (size_t)rr * (NH * DH) + h * DH + c * 64 + ch * 8);
      }
    };
    char* scr = lds + 32768;
    ld_qk(0);
#pragma unroll
    for (int it = 0; it < 4; ++it) {
#pragma unroll
      for (int u = 0; u < 8; ++u) {
        const int cidx = tid + 512 * u, mat = cidx >> 11, rr = (cidx >> 4) & 127, ch = cidx & 15;
        *(u32x4*)(scr + mat * (128 * P1) + rr * P1 + ch * 16) = r[u];
      }
      __syncthreads();
      if (it < 3) ld_qk(it + 1); else ld_wo();
      const char* base = scr + (w >> 2) * (128 * P1);
#pragma unroll
      for (int k4 = 0; k4 < 4; ++k4)
#pragma unroll
        for (int t = 0; t < 2; ++t) {
          const int c16 = 2 * (w & 3) + t;
          bf16x4 lo = tr_read(base, (32 * k4 + 8 * g + q4) * P1 + (16 * c16 + 4 * p4) * 2);
          bf16x4 hi = tr_read(base, (32 * k4 + 8 * g + 4 + q4) * P1 + (16 * c16 + 4 * p4) * 2);
          wqk[t][4 * it + k4] = cat(lo, hi);
        }
      __syncthreads();
    }
    scr = lds;
#pragma unroll
    for (int u = 0; u < 8; ++u) {
      const int cidx = tid + 512 * u, rr = cidx >> 6, ch = cidx & 63;
      *(u32x4*)(scr + rr * P2 + ch * 16) = r[u];
    }
    __syncthreads();
    ld_wv();
#pragma unroll
    for (int nt = 0; nt < 4; ++nt)
#pragma unroll
      for (int k2 = 0; k2 < 2; ++k2) {
        const int col = 64 * w + 16 * nt + 4 * p4;
        bf16x4 lo = tr_read(scr, ((2 * k2 + 0) * 16 + 4 * g + q4) * P2 + col * 2);
        bf16x4 hi = tr_read(scr, ((2 * k2 + 1) * 16 + 4 * g + q4) * P2 + col * 2);
        *(bf16x8*)(lds + CL_WO + (((w * 4 + nt) * 2 + k2) * 64 + lane) * 16) = cat(lo, hi);
      }
    __syncthreads();
#pragma unroll
    for (int it = 0; it < 4; ++it) {
#pragma unroll
      for (int u = 0; u < 2; ++u) *(u32x4*)(scr + (64 * u + (tid >> 3)) * P3 + (tid & 7) * 16) = r[2 * it + u];
      __syncthreads();
#pragma unroll
      for (int e2 = 0; e2 < 2; ++e2) {
        const int et = 2 * (w & 1) + e2, k4 = w >> 1;
        bf16x4 lo = tr_read(scr, (32 * k4 + 8 * g + q4) * P3 + (16 * et + 4 * p4) * 2);
        bf16x4 hi = tr_read(scr, (32 * k4 + 8 * g + 4 + q4) * P3 + (16 * et + 4 * p4) * 2);
        *(bf16x8*)(lds + CL_WV + ((et * 16 + 4 * it + k4) * 64 + lane) * 16) = cat(lo, hi);
      }
      __syncthreads();
    }
  }
  const auto rs_mem = __builtin_amdgcn_make_buffer_rsrc(ws + OFF_MEMH, 0, 65 * 8192, 0x00020000);
  const auto rs_part = __builtin_amdgcn_make_buffer_rsrc(ws + OFF_PART, 0, 1 << 20, 0x00020000);
  const auto rs_km = __builtin_amdgcn_make_buffer_rsrc(ws + OFF_KMH, 0, 16 << 20, 0x00020000);
  const float scale = 0.08838834764831845f;
  const auto rs_kv = __builtin_amdgcn_make_buffer_rsrc(ws + OFF_KF, 0, 32 << 20, 0x00020000);
  unsigned rdy = 0;
  if (w < 4) {
    unsigned spins = 0;
    while (__builtin_amdgcn_readfirstlane(__hip_atomic_load(cnt_ptr(ws, C_KV), RLX_AGENT)) < 64u) { __builtin_amdgcn_s_sleep(4); if (++spins > SPIN_CAP) break; }
    const u32x4 a0 = __builtin_amdgcn_raw_buffer_load_b128(rs_kv, lane * 16, h * 8192 + w * 2048, 16);
    const u32x4 a1 = __builtin_amdgcn_raw_buffer_load_b128(rs_kv, lane * 16, h * 8192 + w * 2048 + 1024, 16);
    const u32x4 a2 = __builtin_amdgcn_raw_buffer_load_b128(rs_kv, lane * 16, (16 << 20) + h * 8192 + c * 4096 + w * 1024, 16);
    *(u32x4*)(lds + lane * 16 + CL_KX + w * 2048) = a0;
    *(u32x4*)(lds + lane * 16 + CL_KX + w * 2048 + 1024) = a1;
    *(u32x4*)(lds + lane * 16 + CL_VX + w * 1024) = a2;
    rdy = 64u;
  }
  const int pst_off = (((8 * w + 2 * (g & 1) + (g >> 1)) * 64 + b) * 8 + li) * 16;
  const char* l16 = lds + lane * 16;
  const char* mfb = lds + CL_MEM + (g * 8 + (li & 7)) * 16;
  const char* wvb = lds + CL_WV + ((w & 3) * 1024 + lane) * 16;
  const char* wob = lds + CL_WO + (w * 512 + lane) * 16;
  for (int s = 0; s < NSEG; ++s) {
    if (w >= 6) {
      u32x4 v[4]; unsigned spins = 0;
      asm volatile("s_waitcnt vmcnt(0)" ::: "memory");
      for (;;) {
        asm volatile("" ::: "memory");
#pragma unroll
        for (int u = 0; u < 4; ++u) v[u] = __builtin_amdgcn_raw_buffer_load_b128(rs_mem, lane * 16, s * 8192 + (w - 6) * 4096 + u * 1024, 16);
        unsigned m = 0;
#pragma unroll
        for (int u = 0; u < 4; ++u) m = max(m, max(v[u][0], v[u][2]));
        if (__all(m != SENT)) break;
        if (++spins > SPIN_CAP) break;
      }
#pragma unroll
      for (int u = 0; u < 4; ++u) *(u32x4*)(l16 + CL_MEM + (w - 6) * 4096 + u * 1024) = v[u];
    }
    STAMP(0);
    __syncthreads();
    STAMP(1);
    if (tid == 256 && s > 0) __hip_atomic_store(cnt_ptr(ws, C_CH + s - 1) + b, 1u, RLX_AGENT);
    u32x4 pf[3];
    const int sn = (s + 1 < NSEG) ? s + 1 : s;
    if (w < 4) {
      if (__builtin_amdgcn_readfirstlane(rdy) < 64u) {
        unsigned spins = 0;
        while (__builtin_amdgcn_readfirstlane(__hip_atomic_load(cnt_ptr(ws, C_KV + (sn >> 2)), RLX_AGENT)) < 64u) { __builtin_amdgcn_s_sleep(2); if (++spins > SPIN_CAP) break; }
      }
      const int so = (sn * NH + h) * 8192;
      pf[0] = __builtin_amdgcn_raw_buffer_load_b128(rs_kv, lane * 16, so + w * 2048, 16);
      pf[1] = __builtin_amdgcn_raw_buffer_load_b128(rs_kv, lane * 16, so + w * 2048 + 1024, 16);
      pf[2] = __builtin_amdgcn_raw_buffer_load_b128(rs_kv, lane * 16, (16 << 20) + so + c * 4096 + w * 1024, 16);
      const int s2 = (s + 2 < NSEG) ? s + 2 : NSEG - 1;
      rdy = __hip_atomic_load(cnt_ptr(ws, C_KV + (s2 >> 2)), RLX_AGENT);
    }
    bf16x8 mf[16];
#pragma unroll
    for (int ks = 0; ks < 16; ++ks) mf[ks] = *(const bf16x8*)(mfb + ks * 512);
    __builtin_amdgcn_sched_barrier(0);
    {
      f32x4 a0 = {0, 0, 0, 0}, a1 = {0, 0, 0, 0};
#pragma unroll
      for (int ks = 0; ks < 16; ++ks) {
        a0 = mfma32(wqk[0][ks], mf[ks], a0);
        a1 = mfma32(wqk[1][ks], mf[ks], a1);
      }
      const bf16x8 o = cat(pk4(a0), pk4(a1));
      *(bf16x8*)(lds + (w < 4 ? CL_QF : CL_KF) + ((w & 3) * 64 + lane) * 16) = o;
      if (w >= 4 && c == 0) __builtin_amdgcn_raw_buffer_store_b128(__builtin_bit_cast(u32x4, o), rs_km, lane * 16, ((s * NH + h) * 4 + (w & 3)) * 1024, 16);
    }
    STAMP(2);
    bf16x4 vm4 = {(__bf16)0.f, (__bf16)0.f, (__bf16)0.f, (__bf16)0.f};
    if (w == 0) {
      bf16x8 kxf[8];
#pragma unroll
      for (int u = 0; u < 8; ++u) kxf[u] = *(const bf16x8*)(l16 + CL_KX + u * 1024);
      __syncthreads();
      f32x4 s0 = {0, 0, 0, 0}, s1 = {0, 0, 0, 0}, s2 = {0, 0, 0, 0};
#pragma unroll
      for (int kk = 0; kk < 4; ++kk) {
        const bf16x8 qf = *(const bf16x8*)(l16 + CL_QF + kk * 1024);
        const bf16x8 k0 = *(const bf16x8*)(l16 + CL_KF + kk * 1024);
        s0 = mfma32(k0, qf, s0); s1 = mfma32(kxf[kk], qf, s1); s2 = mfma32(kxf[4 + kk], qf, s2);
      }
      if (g >= 2) s0 = (f32x4){-3.0e38f, -3.0e38f, -3.0e38f, -3.0e38f};
      float m = fmaxf(fmaxf(fmaxf(s0[0], s0[1]), fmaxf(s0[2], s0[3])), fmaxf(fmaxf(fmaxf(s1[0], s1[1]), fmaxf(s1[2], s1[3])), fmaxf(fmaxf(s2[0], s2[1]), fmaxf(s2[2], s2[3]))));
      m = xmax16_32(m);
      const float cexp = scale * 1.4426950408889634f, mc = -m * cexp;
      bf16x4 p0; bf16x8 p12;
#pragma unroll
      for (int r = 0; r < 4; ++r) {
        p0[r] = (__bf16)__builtin_amdgcn_exp2f(fmaf(s0[r], cexp, mc));
        p12[r] = (__bf16)__builtin_amdgcn_exp2f(fmaf(s1[r], cexp, mc));
        p12[4 + r] = (__bf16)__builtin_amdgcn_exp2f(fmaf(s2[r], cexp, mc));
      }
      *(bf16x8*)(lds + CL_P + lane * 16) = p12;
      *(bf16x4*)(lds + CL_P + 1024 + lane * 8) = p0;
    } else {
      if (w >= 4) {
        f32x4 va = {0, 0, 0, 0}, vb = {0, 0, 0, 0};
        bf16x8 wv[2][4];
#pragma unroll
        for (int k1 = 0; k1 < 4; ++k1) wv[0][k1] = *(const bf16x8*)(wvb + k1 * 1024);
        __syncthreads();
#pragma unroll
        for (int k4 = 0; k4 < 4; ++k4) {
          if (k4 < 3) {
#pragma unroll
            for (int k1 = 0; k1 < 4; ++k1) wv[(k4 + 1) & 1][k1] = *(const bf16x8*)(wvb + (4 * k4 + 4 + k1) * 1024);
          }
#pragma unroll
          for (int k1 = 0; k1 < 4; k1 += 2) { va = mfma32(mf[4 * k4 + k1], wv[k4 & 1][k1], va); vb = mfma32(mf[4 * k4 + k1 + 1], wv[k4 & 1][k1 + 1], vb); }
          __builtin_amdgcn_sched_barrier(0);
        }
        vm4 = pk4(va + vb);
        __builtin_amdgcn_raw_buffer_store_b64(__builtin_bit_cast(u32x2, vm4), rs_km, lane * 8, (8 << 20) + ((s * NH + h) * 8 + 4 * c + (w - 4)) * 512, 16);
      } else {
        __syncthreads();
      }
    }
    bf16x8 wo[4][2];
#pragma unroll
    for (int nt = 0; nt < 4; ++nt)
#pragma unroll
      for (int k2 = 0; k2 < 2; ++k2) wo[nt][k2] = *(const bf16x8*)(wob + (nt * 2 + k2) * 1024);
    __syncthreads();
    if (w >= 4) {
      const bf16x8 p12 = *(const bf16x8*)(lds + CL_P + lane * 16);
      const bf16x4 p0 = *(const bf16x4*)(lds + CL_P + 1024 + lane * 8);
      const bf16x8 vx8 = *(const bf16x8*)(lds + CL_VX + ((w - 4) * 64 + lane) * 16);
      f32x4 o = mfma16(vm4, p0, (f32x4){0, 0, 0, 0});
      const bf16x4 one4 = {(__bf16)1.f, (__bf16)1.f, (__bf16)1.f, (__bf16)1.f};
      f32x4 l = mfma16(one4, p0, (f32x4){0, 0, 0, 0});
      o = mfma32(vx8, p12, o);
      l = mfma32(cat(one4, one4), p12, l);
      const float inv = __builtin_amdgcn_rcpf(l[0]);
      *(bf16x4*)(lds + CL_OF + (((w - 4) >> 1) * 64 + lane) * 16 + (w & 1) * 8) = pk4(o * inv);
    }
    STAMP(3);
    __syncthreads();
    STAMP(4);
    if (s + 1 == NSEG) {
      if (w >= 4) asm volatile("s_waitcnt vmcnt(0)" ::: "memory");
      __syncthreads();
      if (tid == 64) __hip_atomic_store(cnt_ptr(ws, C_CH + s) + b, 1u, RLX_AGENT);
      break;
    }
    const int slot = (s & 1) << 19;
    {
      const bf16x8 of0 = *(const bf16x8*)(l16 + CL_OF);
      const bf16x8 of1 = *(const bf16x8*)(l16 + CL_OF + 1024);
#pragma unroll
      for (int n2 = 0; n2 < 2; ++n2) {
        f32x4 pa = mfma32(wo[2 * n2][0], of0, (f32x4){0, 0, 0, 0});
        f32x4 pb = mfma32(wo[2 * n2 + 1][0], of0, (f32x4){0, 0, 0, 0});
        pa = mfma32(wo[2 * n2][1], of1, pa);
        pb = mfma32(wo[2 * n2 + 1][1], of1, pb);
        u32x2 A = __builtin_bit_cast(u32x2, pk4(pa)), B = __builtin_bit_cast(u32x2, pk4(pb));
        auto r0 = __builtin_amdgcn_permlane16_swap(A[0], B[0], false, false);
        auto r1 = __builtin_amdgcn_permlane16_swap(A[1], B[1], false, false);
        u32x4 pv = {r0[0] | 1u, r1[0], r0[1] | 1u, r1[1]};
        if (li < 8) __builtin_amdgcn_raw_buffer_store_b128(pv, rs_part, pst_off, slot + n2 * 32768, 16);
      }
    }
    STAMP(5);
    if (w < 4) {
      *(u32x4*)(l16 + CL_KX + w * 2048) = pf[0];
      *(u32x4*)(l16 + CL_KX + w * 2048 + 1024) = pf[1];
      *(u32x4*)(l16 + CL_VX + w * 1024) = pf[2];
    }
    if (w == 4 || w == 5) asm volatile("s_waitcnt vmcnt(0)" ::: "memory");
    if (w >= 6) {
      u32x2 v[8]; unsigned spins = 0;
      const int roff = lane * 16 + (w - 6) * 8;
      asm volatile("s_waitcnt vmcnt(0)" ::: "memory");
      for (;;) {
        asm volatile("" ::: "memory");
#pragma unroll
        for (int it = 0; it < 8; ++it) v[it] = __builtin_amdgcn_raw_buffer_load_b64(rs_part, roff, slot + b * 8192 + it * 1024, 16);
        unsigned m = 1u;
#pragma unroll
        for (int it = 0; it < 8; ++it) m &= v[it][0];
        if (__all(m != 0u)) break;
        if (++spins > SPIN_CAP) break;
      }
      STAMP(6);
      f32x4 a = {0, 0, 0, 0};
#pragma unroll
      for (int it = 0; it < 8; ++it) {
        a[0] += __builtin_bit_cast(float, v[it][0] << 16); a[1] += __builtin_bit_cast(float, v[it][0] & 0xffff0000u);
        a[2] += __builtin_bit_cast(float, v[it][1] << 16); a[3] += __builtin_bit_cast(float, v[it][1] & 0xffff0000u);
      }
#pragma unroll
      for (int r = 0; r < 4; ++r) a[r] = xsum8_16_32(a[r]);
      u32x2 o = __builtin_bit_cast(u32x2, pk4(a));
      if (o[0] == SENT) o[0] = 0x7FFF7FFFu;
      if (lane < 8) __builtin_amdgcn_raw_buffer_store_b64(o, rs_mem, lane * 16 + (w - 6) * 8, (s + 1) * 8192 + b * 128, 16);
      const u32x2 z = {0u, 0u};
#pragma unroll
      for (int it = 0; it < 8; ++it) __builtin_amdgcn_raw_buffer_store_b64(z, rs_part, roff, slot + b * 8192 + it * 1024, 16);
      STAMP(7);
    }
  }
}

__global__ __launch_bounds__(512, 2) void k_main(const bf16* __restrict__ x, const bf16* __restrict__ Wq, const bf16* __restrict__ Wk,
                                                 const bf16* __restrict__ Wv, const bf16* __restrict__ Wo, const bf16* __restrict__ Wg,
                                                 const bf16* __restrict__ Wu, const bf16* __restrict__ Wd, bf16* __restrict__ out, char* ws) {
  extern __shared__ __attribute__((aligned(16))) char lds[];
  if (blockIdx.x < 64) chain(Wq, Wk, Wv, Wo, ws, lds);
  else worker(x, Wq, Wk, Wv, Wo, Wg, Wu, Wd, out, ws, lds);
}

extern "C" void kernel_launch(void* const* d_in, const int* in_sizes, int n_in,
                              void* d_out, int out_size, void* d_ws, size_t ws_size,
                              hipStream_t stream) {
  const bf16* x = (const bf16*)d_in[0];
  const bf16* mem0 = (const bf16*)d_in[1];
  const bf16* Wq = (const bf16*)d_in[2];
  const bf16* Wk = (const bf16*)d_in[3];
  const bf16* Wv = (const bf16*)d_in[4];
  const bf16* Wo = (const bf16*)d_in[5];
  const bf16* Wg = (const bf16*)d_in[8];
  const bf16* Wu = (const bf16*)d_in[9];
  const bf16* Wd = (const bf16*)d_in[10];
  char* ws = (char*)d_ws;
  (void)hipFuncSetAttribute((const void*)k_main, hipFuncAttributeMaxDynamicSharedMemorySize, CHAIN_LDS);
  k_ln<<<S_ / 4, 256, 0, stream>>>(x, mem0, ws);
  k_main<<<256, 512, CHAIN_LDS, stream>>>(x, Wq, Wk, Wv, Wo, Wg, Wu, Wd, (bf16*)d_out, ws);
}
```

```cpp
#include <hip/hip_runtime.h>
#include <hip/hip_bf16.h>

constexpr int S_ = 2048, DM = 512, NH = 32, DH = 128, MM = 8, SEG = 32, NSEG = 64, FF = 1792;

typedef __bf16 bf16;
typedef __bf16 bf16x8 __attribute__((ext_vector_type(8)));
typedef __bf16 bf16x4 __attribute__((ext_vector_type(4)));
typedef short s16x4 __attribute__((ext_vector_type(4)));
typedef float f32x4 __attribute__((ext_vector_type(4)));
typedef unsigned u32x4 __attribute__((ext_vector_type(4)));
typedef unsigned u32x2 __attribute__((ext_vector_type(2)));
#define LAS __attribute__((address_space(3)))

constexpr size_t OFF_MEMH = 0;
constexpr size_t OFF_PART = 1 << 20;
constexpr size_t OFF_CNT  = 2 << 20;
constexpr size_t OFF_XN   = 4u << 20;
constexpr size_t OFF_BOUT = 6u << 20;
constexpr size_t OFF_H    = 8u << 20;
constexpr size_t OFF_QF   = 16u << 20;
constexpr size_t OFF_KF   = 32u << 20;
constexpr size_t OFF_VF   = 48u << 20;
constexpr size_t OFF_OX   = 64u << 20;
constexpr size_t OFF_KMH  = 80u << 20;
constexpr size_t OFF_VMH  = 88u << 20;
constexpr size_t OFF_WOF  = 96u << 20;
constexpr unsigned WS_SPAN = 100u << 20;
constexpr int C_TICKET = 0, C_KV = 1, C_Q = 17, C_H = 33, C_BO = 49, C_WOF = 65, C_X = 66, C_CH = 130, C_F = 194;
#define RLX_AGENT __ATOMIC_RELAXED, __HIP_MEMORY_SCOPE_AGENT

__device__ __forceinline__ f32x4 mfma32(bf16x8 a, bf16x8 b, f32x4 c) {
  return __builtin_amdgcn_mfma_f32_16x16x32_bf16(a, b, c, 0, 0, 0);
}
__device__ __forceinline__ f32x4 mfma16(bf16x4 a, bf16x4 b, f32x4 c) {
  bf16x8 a8, b8;
#pragma unroll
  for (int j = 0; j < 4; ++j) { a8[j] = a[j]; b8[j] = b[j]; a8[4 + j] = (__bf16)0.f; b8[4 + j] = (__bf16)0.f; }
  return __builtin_amdgcn_mfma_f32_16x16x32_bf16(a8, b8, c, 0, 0, 0);
}
__device__ __forceinline__ float bfr(float x) { return (float)(__bf16)x; }
__device__ __forceinline__ bf16x4 tr_read(const char* lds_base, unsigned off) {
  s16x4 t = __builtin_amdgcn_ds_read_tr16_b64_v4i16((LAS s16x4*)(lds_base + off));
  return __builtin_bit_cast(bf16x4, t);
}
__device__ __forceinline__ bf16x8 cat(bf16x4 a, bf16x4 b) {
  bf16x8 r; r[0] = a[0]; r[1] = a[1]; r[2] = a[2]; r[3] = a[3]; r[4] = b[0]; r[5] = b[1]; r[6] = b[2]; r[7] = b[3]; return r;
}
__device__ __forceinline__ bf16x4 pk4(f32x4 v) { bf16x4 r; r[0] = (__bf16)v[0]; r[1] = (__bf16)v[1]; r[2] = (__bf16)v[2]; r[3] = (__bf16)v[3]; return r; }

__device__ __forceinline__ float xmax16_32(float x) {
  unsigned u = __builtin_bit_cast(unsigned, x);
  auto a = __builtin_amdgcn_permlane16_swap(u, u, false, false);
  x = fmaxf(__builtin_bit_cast(float, (unsigned)a[0]), __builtin_bit_cast(float, (unsigned)a[1]));
  u = __builtin_bit_cast(unsigned, x);
  auto c = __builtin_amdgcn_permlane32_swap(u, u, false, false);
  return fmaxf(__builtin_bit_cast(float, (unsigned)c[0]), __builtin_bit_cast(float, (unsigned)c[1]));
}
__device__ __forceinline__ float xsum8_16_32(float x) {
  x += __builtin_bit_cast(float, __builtin_amdgcn_update_dpp(0, __builtin_bit_cast(int, x), 0x128, 0xf, 0xf, false));
  unsigned u = __builtin_bit_cast(unsigned, x);
  auto a = __builtin_amdgcn_permlane16_swap(u, u, false, false);
  x = __builtin_bit_cast(float, (unsigned)a[0]) + __builtin_bit_cast(float, (unsigned)a[1]);
  u = __builtin_bit_cast(unsigned, x);
  auto c = __builtin_amdgcn_permlane32_swap(u, u, false, false);
  return __builtin_bit_cast(float, (unsigned)c[0]) + __builtin_bit_cast(float, (unsigned)c[1]);
}

__global__ __launch_bounds__(256) void k_ln(const bf16* __restrict__ x, const bf16* __restrict__ mem0, char* ws) {
  const int lane = threadIdx.x & 63, row = blockIdx.x * 4 + (threadIdx.x >> 6);
  bf16* xn = (bf16*)(ws + OFF_XN);
  bf16x8 v = *(const bf16x8*)(x + (size_t)row * DM + lane * 8);
  float f[8], s = 0.f;
#pragma unroll
  for (int j = 0; j < 8; ++j) { f[j] = (float)v[j]; s += f[j]; }
#pragma unroll
  for (int m = 32; m >= 1; m >>= 1) s += __shfl_xor(s, m);
  const float mean = s * (1.f / DM);
  float q = 0.f;
#pragma unroll
  for (int j = 0; j < 8; ++j) { float d = f[j] - mean; q += d * d; }
#pragma unroll
  for (int m = 32; m >= 1; m >>= 1) q += __shfl_xor(q, m);
  const float mb = bfr(mean), vb = bfr(q * (1.f / DM));
  const float den = bfr(sqrtf(bfr(vb + 1e-5f)));
  bf16x8 o;
#pragma unroll
  for (int j = 0; j < 8; ++j) o[j] = (__bf16)(bfr(f[j] - mb) / den);
  *(bf16x8*)(xn + (size_t)row * DM + lane * 8) = o;
  {
    const u32x4 ff = {0xFFFFFFFFu, 0xFFFFFFFFu, 0xFFFFFFFFu, 0xFFFFFFFFu}, zz = {0u, 0u, 0u, 0u};
    if (threadIdx.x < 64) *(u32x4*)(ws + OFF_MEMH + 8192 + ((size_t)blockIdx.x * 64 + threadIdx.x) * 16) = ff;
    if (threadIdx.x < 136) *(u32x4*)(ws + OFF_PART + ((size_t)blockIdx.x * 136 + threadIdx.x) * 16) = zz;
  }
  if (blockIdx.x == 0) {
    for (int c = threadIdx.x; c < 512; c += 256) {
      const int i = c & 7, dg = c >> 3;
      *(bf16x8*)(ws + OFF_MEMH + (size_t)c * 16) = *(const bf16x8*)(mem0 + i * DM + dg * 8);
    }
  }
}

constexpr int GA_PITCH = 272;
constexpr int GA_BYTES = 128 * GA_PITCH;
constexpr int GB_BYTES = 128 * 256;
__device__ __forceinline__ unsigned off_b(unsigned row, unsigned ch) { return 256u * row + 16u * (ch ^ (((row & 3) << 2) | ((row >> 2) & 3))); }
typedef __amdgpu_buffer_rsrc_t rsrc_t;
__device__ __forceinline__ void st16(rsrc_t rs, unsigned off, bf16x8 v) { __builtin_amdgcn_raw_buffer_store_b128(__builtin_bit_cast(u32x4, v), rs, off, 0, 16); }
__device__ __forceinline__ void st8(rsrc_t rs, unsigned off, bf16x4 v) { __builtin_amdgcn_raw_buffer_store_b64(__builtin_bit_cast(u32x2, v), rs, off, 0, 16); }

__device__ __forceinline__ bf16x8 ld16(rsrc_t rs, unsigned voff, unsigned soff) { return __builtin_bit_cast(bf16x8, __builtin_amdgcn_raw_buffer_load_b128(rs, voff, soff, 16)); }
__device__ __forceinline__ bf16x4 ld8(rsrc_t rs, unsigned voff, unsigned soff) { return __builtin_bit_cast(bf16x4, __builtin_amdgcn_raw_buffer_load_b64(rs, voff, soff, 16)); }

template <int MODE>
__device__ __forceinline__ void gemm_tile(const bf16* __restrict__ A, int lda, const bf16* __restrict__ B0, const bf16* __restrict__ B1,
                                          int ldb, int K, int row0, int col0, char* lds, rsrc_t rs, unsigned dst, int tid) {
  const int lane = tid & 63, w = tid >> 6, wm = w >> 1, wn = w & 1;
  const int li = lane & 15, g = lane >> 4, q = li >> 2, p = li & 3;
  char* la = lds; char* lb = lds + GA_BYTES;
  f32x4 acc[2][4];
#pragma unroll
  for (int a = 0; a < 2; ++a)
#pragma unroll
    for (int b = 0; b < 4; ++b) acc[a][b] = (f32x4){0.f, 0.f, 0.f, 0.f};
  const bf16* asrc[4]; const bf16* bsrc[4]; unsigned adst[4], bdst[4];
#pragma unroll
  for (int u = 0; u < 4; ++u) {
    const int c = tid + 512 * u;
    asrc[u] = A + (size_t)(row0 + (c >> 4)) * lda + (c & 15) * 8;
    adst[u] = (c >> 4) * GA_PITCH + (c & 15) * 16;
    const int kr = c >> 4, ch = c & 15;
    if (MODE == 2) {
      const int blk = ch >> 2;
      bsrc[u] = ((blk & 1) ? B1 : B0) + (size_t)kr * ldb + col0 + (blk >> 1) * 32 + (ch & 3) * 8;
    } else {
      bsrc[u] = B0 + (size_t)kr * ldb + col0 + ch * 8;
    }
    bdst[u] = off_b(kr, ch);
  }
  auto compute = [&]() {
#pragma unroll
    for (int ks = 0; ks < 4; ++ks) {
      bf16x8 af[2], bfr_[4];
#pragma unroll
      for (int tt = 0; tt < 2; ++tt) af[tt] = *(const bf16x8*)(la + (wm * 32 + tt * 16 + li) * GA_PITCH + ks * 64 + g * 16);
#pragma unroll
      for (int nt = 0; nt < 4; ++nt) {
        const unsigned ch = 2 * (wn * 4 + nt) + (p >> 1);
        bf16x4 lo = tr_read(lb, off_b(32 * ks + 8 * g + q, ch) + 8 * (p & 1));
        bf16x4 hi = tr_read(lb, off_b(32 * ks + 8 * g + 4 + q, ch) + 8 * (p & 1));
        bfr_[nt] = cat(lo, hi);
      }
#pragma unroll
      for (int tt = 0; tt < 2; ++tt)
#pragma unroll
        for (int nt = 0; nt < 4; ++nt) {
          if (MODE == 1) acc[tt][nt] = mfma32(af[tt], bfr_[nt], acc[tt][nt]);
          else acc[tt][nt] = mfma32(bfr_[nt], af[tt], acc[tt][nt]);
        }
    }
  };
  {
    u32x4 ra[4], rb[4];
#pragma unroll
    for (int u = 0; u < 4; ++u) { ra[u] = *(const u32x4*)asrc[u]; rb[u] = *(const u32x4*)bsrc[u]; }
    const int nkt = K >> 7;
    for (int kt = 0; kt < nkt; ++kt) {
#pragma unroll
      for (int u = 0; u < 4; ++u) { *(u32x4*)(la + adst[u]) = ra[u]; *(u32x4*)(lb + bdst[u]) = rb[u]; }
      __syncthreads();
      if (kt + 1 < nkt) {
#pragma unroll
        for (int u = 0; u < 4; ++u) {
          ra[u] = *(const u32x4*)(asrc[u] + (kt + 1) * 128);
          rb[u] = *(const u32x4*)(bsrc[u] + (size_t)(kt + 1) * 128 * ldb);
        }
      }
      compute();
      __syncthreads();
    }
  }
  if (MODE == 0) {
    const int seg = (row0 >> 5) + wm, head = col0 >> 7;
#pragma unroll
    for (int tt = 0; tt < 2; ++tt)
#pragma unroll
      for (int k2 = 0; k2 < 2; ++k2)
        st16(rs, dst + ((((seg * NH + head) * 2 + tt) * 4 + (wn * 2 + k2)) * 64 + lane) * 16, cat(pk4(acc[tt][2 * k2]), pk4(acc[tt][2 * k2 + 1])));
  } else if (MODE == 1) {
    const int seg = (row0 >> 5) + wm, head = col0 >> 7;
#pragma unroll
    for (int nt = 0; nt < 4; ++nt)
      st16(rs, dst + (((seg * NH + head) * 8 + (wn * 4 + nt)) * 64 + lane) * 16, cat(pk4(acc[0][nt]), pk4(acc[1][nt])));
  } else if (MODE == 2) {
#pragma unroll
    for (int tt = 0; tt < 2; ++tt)
#pragma unroll
      for (int n2 = 0; n2 < 2; ++n2) {
        f32x4 hv;
#pragma unroll
        for (int r = 0; r < 4; ++r) {
          const float gg = bfr(acc[tt][n2][r]), uu = bfr(acc[tt][n2 + 2][r]);
          const float sg = bfr(1.f / (1.f + __expf(-gg)));
          hv[r] = sg * uu;
        }
        const int row = row0 + wm * 32 + tt * 16 + li, col = col0 + 32 * wn + 16 * n2 + 4 * g;
        st8(rs, dst + (row * FF + col) * 2, pk4(hv));
      }
  } else {
#pragma unroll
    for (int tt = 0; tt < 2; ++tt)
#pragma unroll
      for (int nt = 0; nt < 4; ++nt) {
        const int row = row0 + wm * 32 + tt * 16 + li, col = col0 + 64 * wn + 16 * nt + 4 * g;
        st8(rs, dst + (row * DM + col) * 2, pk4(acc[tt][nt]));
      }
  }
}

__device__ __forceinline__ unsigned* cnt_ptr(char* ws, int idx) { return (unsigned*)(ws + OFF_CNT) + idx * 64; }
__device__ __forceinline__ void wait_cnt(char* ws, int idx, unsigned target) {
  unsigned spins = 0;
  while (__hip_atomic_load(cnt_ptr(ws, idx), RLX_AGENT) < target) { __builtin_amdgcn_s_sleep(4); if (++spins > (1u << 18)) break; }
}
__device__ __forceinline__ void acquire_all() {
  if (threadIdx.x == 0) { __builtin_amdgcn_fence(__ATOMIC_ACQUIRE, "agent"); asm volatile("s_waitcnt vmcnt(0)" ::: "memory"); }
  __syncthreads();
}
__device__ __forceinline__ void signal_cnt(char* ws, int idx) {
  asm volatile("s_waitcnt vmcnt(0)" ::: "memory");
  __syncthreads();
  if (threadIdx.x == 0) __hip_atomic_fetch_add(cnt_ptr(ws, idx), 1u, RLX_AGENT);
}

__device__ __forceinline__ void xattn_task(char* ws, rsrc_t rs, int seg, int head, int tid) {
  const int lane = tid & 63, li = lane & 15, g = lane >> 4;
  const int unit = seg * NH + head, rb = seg >> 2;
  const unsigned qf = (unsigned)OFF_QF + unit * 8192, kf = (unsigned)OFF_KF + unit * 8192, vf = (unsigned)OFF_VF + unit * 8192;
  const unsigned km = (unsigned)OFF_KMH + unit * 4096, vm = (unsigned)OFF_VMH + unit * 4096;
  const float scale = 0.08838834764831845f;
  if (threadIdx.x == 0) { wait_cnt(ws, C_Q + rb, 32); wait_cnt(ws, C_KV + rb, 64); }
  __syncthreads();
  f32x4 sc[3][2];
#pragma unroll
  for (int a = 0; a < 3; ++a) { sc[a][0] = (f32x4){0, 0, 0, 0}; sc[a][1] = (f32x4){0, 0, 0, 0}; }
  bf16x8 q[2][4], vx[8];
#pragma unroll
  for (int kk = 0; kk < 4; ++kk) {
    q[0][kk] = ld16(rs, lane * 16, qf + kk * 1024); q[1][kk] = ld16(rs, lane * 16, qf + 4096 + kk * 1024);
    const bf16x8 k1 = ld16(rs, lane * 16, kf + kk * 1024), k2 = ld16(rs, lane * 16, kf + 4096 + kk * 1024);
    sc[1][0] = mfma32(k1, q[0][kk], sc[1][0]); sc[1][1] = mfma32(k1, q[1][kk], sc[1][1]);
    sc[2][0] = mfma32(k2, q[0][kk], sc[2][0]); sc[2][1] = mfma32(k2, q[1][kk], sc[2][1]);
  }
#pragma unroll
  for (int et = 0; et < 8; ++et) vx[et] = ld16(rs, lane * 16, vf + et * 1024);
  if (threadIdx.x < 64) {
    unsigned spins = 0;
    while (!__all(__hip_atomic_load(cnt_ptr(ws, C_CH + seg) + threadIdx.x, RLX_AGENT) != 0u)) { __builtin_amdgcn_s_sleep(4); if (++spins > (1u << 18)) break; }
  }
  __syncthreads();
  bf16x8 k0[4]; bf16x4 vmf[8];
#pragma unroll
  for (int kk = 0; kk < 4; ++kk) k0[kk] = ld16(rs, lane * 16, km + kk * 1024);
#pragma unroll
  for (int et = 0; et < 8; ++et) vmf[et] = ld8(rs, lane * 8, vm + et * 512);
#pragma unroll
  for (int kk = 0; kk < 4; ++kk) { sc[0][0] = mfma32(k0[kk], q[0][kk], sc[0][0]); sc[0][1] = mfma32(k0[kk], q[1][kk], sc[0][1]); }
  bf16x4 p0[2]; bf16x8 p12[2];
#pragma unroll
  for (int qt = 0; qt < 2; ++qt) {
    const int qi = 16 * qt + li;
    float v[12]; float m = -3.0e38f;
#pragma unroll
    for (int kt = 0; kt < 3; ++kt)
#pragma unroll
      for (int r = 0; r < 4; ++r) {
        float s = bfr(bfr(sc[kt][qt][r]) * scale);
        bool ok = (kt == 0) ? (g < 2) : (16 * (kt - 1) + 4 * g + r <= qi);
        s = ok ? s : -3.0e38f;
        v[kt * 4 + r] = s; m = fmaxf(m, s);
      }
    m = xmax16_32(m);
    float sum = 0.f;
#pragma unroll
    for (int e = 0; e < 12; ++e) { v[e] = (v[e] > -1.0e38f) ? __expf(v[e] - m) : 0.f; sum += v[e]; }
    sum += __shfl_xor(sum, 16); sum += __shfl_xor(sum, 32);
    const float inv = 1.f / sum;
#pragma unroll
    for (int r = 0; r < 4; ++r) { p0[qt][r] = (__bf16)(v[r] * inv); p12[qt][r] = (__bf16)(v[4 + r] * inv); p12[qt][4 + r] = (__bf16)(v[8 + r] * inv); }
  }
#pragma unroll
  for (int kk = 0; kk < 4; ++kk) {
    f32x4 o[2][2];
#pragma unroll
    for (int e2 = 0; e2 < 2; ++e2)
#pragma unroll
      for (int qt = 0; qt < 2; ++qt) {
        o[qt][e2] = mfma16(vmf[2 * kk + e2], p0[qt], (f32x4){0, 0, 0, 0});
        o[qt][e2] = mfma32(vx[2 * kk + e2], p12[qt], o[qt][e2]);
      }
#pragma unroll
    for (int qt = 0; qt < 2; ++qt)
      st16(rs, (unsigned)OFF_OX + ((((seg * 2 + qt) * NH + head) * 4 + kk) * 64 + lane) * 16, cat(pk4(o[qt][0]), pk4(o[qt][1])));
  }
  signal_cnt(ws, C_X + seg);
}

__device__ __forceinline__ void fastkv_task(const bf16* __restrict__ xn, const bf16* __restrict__ Wk, const bf16* __restrict__ Wv,
                                            char* lds, rsrc_t rs, int t, int tid) {
  const int lane = tid & 63, w = tid >> 6, li = lane & 15, g = lane >> 4, q4 = li >> 2, p4 = li & 3;
  const int isv = t >> 6, head = (t >> 1) & 31, hh = t & 1, tt = w >> 2, nt = w & 3;
  const bf16* W = (isv ? Wv : Wk) + head * DH + hh * 64;
  constexpr int P = 144;
#pragma unroll
  for (int u = 0; u < 8; ++u) {
    const int rr = 64 * u + (tid >> 3), ch = tid & 7;
    *(u32x4*)(lds + rr * P + ch * 16) = *(const u32x4*)(W + (size_t)rr * (NH * DH) + ch * 8);
  }
  bf16x8 af[16];
#pragma unroll
  for (int ks = 0; ks < 16; ++ks) af[ks] = *(const bf16x8*)(xn + (size_t)(16 * tt + li) * DM + 32 * ks + 8 * g);
  __syncthreads();
  f32x4 acc = {0, 0, 0, 0};
#pragma unroll
  for (int ks = 0; ks < 16; ++ks) {
    const bf16x8 bfr_ = cat(tr_read(lds, (32 * ks + 8 * g + q4) * P + (16 * nt + 4 * p4) * 2), tr_read(lds, (32 * ks + 8 * g + 4 + q4) * P + (16 * nt + 4 * p4) * 2));
    if (isv) acc = mfma32(af[ks], bfr_, acc); else acc = mfma32(bfr_, af[ks], acc);
  }
  if (isv) st8(rs, (unsigned)OFF_VF + ((head * 8 + hh * 4 + nt) * 64 + lane) * 16 + tt * 8, pk4(acc));
  else st8(rs, (unsigned)OFF_KF + (((head * 2 + tt) * 4 + hh * 2 + (nt >> 1)) * 64 + lane) * 16 + (nt & 1) * 8, pk4(acc));
}

__device__ __forceinline__ void wof_task(const bf16* __restrict__ Wo, char* lds, rsrc_t rs, int t, int tid) {
  const int lane = tid & 63, w = tid >> 6, li = lane & 15, g = lane >> 4, q4 = li >> 2, p4 = li & 3;
  const int h = t >> 1, chh = t & 1;
  constexpr int P = 528;
#pragma unroll
  for (int u = 0; u < 8; ++u) {
    const int cidx = tid + 512 * u, rr = cidx >> 5, ch = cidx & 31;
    *(u32x4*)(lds + rr * P + ch * 16) = *(const u32x4*)(Wo + ((size_t)h * DH + rr) * DM + chh * 256 + ch * 8);
  }
  __syncthreads();
#pragma unroll
  for (int n2 = 0; n2 < 2; ++n2)
#pragma unroll
    for (int kk = 0; kk < 4; ++kk) {
      const int ntl = 2 * w + n2;
      bf16x4 lo = tr_read(lds, ((2 * kk + 0) * 16 + 4 * g + q4) * P + (16 * ntl + 4 * p4) * 2);
      bf16x4 hi = tr_read(lds, ((2 * kk + 1) * 16 + 4 * g + q4) * P + (16 * ntl + 4 * p4) * 2);
      st16(rs, (unsigned)OFF_WOF + ((((chh * 16 + ntl) * NH + h) * 4 + kk) * 64 + lane) * 16, cat(lo, hi));
    }
}

__device__ __forceinline__ void final_task(char* ws, rsrc_t rs, char* lds, const bf16* __restrict__ x, bf16* __restrict__ out, int seg, int cb, int tid) {
  const int lane = tid & 63, w = __builtin_amdgcn_readfirstlane(tid >> 6);
  if (tid == 0) { wait_cnt(ws, C_WOF, 64); wait_cnt(ws, C_BO + (seg >> 2), 4); }
  __syncthreads();
  bf16x8 bf[16][2];
#pragma unroll
  for (int i = 0; i < 16; ++i)
#pragma unroll
    for (int nt = 0; nt < 2; ++nt) bf[i][nt] = ld16(rs, lane * 16, (unsigned)OFF_WOF + ((cb * 2 + nt) * 128 + w * 16 + i) * 1024);
  const int tt_ = (tid >> 7) & 1, nt_ = (tid >> 6) & 1, li = lane & 15, g = lane >> 4;
  const int row = seg * 32 + tt_ * 16 + li, col = cb * 32 + nt_ * 16 + 4 * g;
  const bf16x4 xv = *(const bf16x4*)(x + (size_t)row * DM + col);
  const bf16x4 bv = ld8(rs, (row * DM + col) * 2, (unsigned)OFF_BOUT);
  if (tid == 0) wait_cnt(ws, C_X + seg, 4);
  __syncthreads();
  f32x4 acc[2][2];
#pragma unroll
  for (int a = 0; a < 2; ++a) { acc[a][0] = (f32x4){0.f, 0.f, 0.f, 0.f}; acc[a][1] = (f32x4){0.f, 0.f, 0.f, 0.f}; }
#pragma unroll
  for (int i4 = 0; i4 < 4; ++i4) {
    bf16x8 af[4][2];
#pragma unroll
    for (int i = 0; i < 4; ++i)
#pragma unroll
      for (int tt = 0; tt < 2; ++tt) af[i][tt] = ld16(rs, lane * 16, (unsigned)OFF_OX + ((seg * 2 + tt) * 128 + w * 16 + i4 * 4 + i) * 1024);
#pragma unroll
    for (int i = 0; i < 4; ++i)
#pragma unroll
      for (int tt = 0; tt < 2; ++tt)
#pragma unroll
        for (int nt = 0; nt < 2; ++nt) acc[tt][nt] = mfma32(bf[i4 * 4 + i][nt], af[i][tt], acc[tt][nt]);
    if (i4 == 1) __builtin_amdgcn_sched_barrier(0);
  }
#pragma unroll
  for (int tt = 0; tt < 2; ++tt)
#pragma unroll
    for (int nt = 0; nt < 2; ++nt) *(f32x4*)(lds + (((w * 2 + tt) * 2 + nt) * 64 + lane) * 16) = acc[tt][nt];
  __syncthreads();
  if (tid < 256) {
    f32x4 a = {0, 0, 0, 0};
#pragma unroll
    for (int ww = 0; ww < 8; ++ww) a += *(const f32x4*)(lds + (((ww * 2 + tt_) * 2 + nt_) * 64 + lane) * 16);
    f32x4 o;
#pragma unroll
    for (int r = 0; r < 4; ++r) o[r] = bfr((float)xv[r] + bfr(a[r])) + (float)bv[r];
    *(bf16x4*)(out + (size_t)row * DM + col) = pk4(o);
  }
}

constexpr int T_F = 0, T_KV = 128, T_Q = T_KV + 1024, T_G = T_Q + 512, T_W = T_G + 448, T_B = T_W + 64, T_S = T_B + 64, T_END = T_S + 64 * 20;

__device__ __forceinline__ void worker(const bf16* __restrict__ x, const bf16* __restrict__ Wq, const bf16* __restrict__ Wk, const bf16* __restrict__ Wv,
                                       const bf16* __restrict__ Wo, const bf16* __restrict__ Wg, const bf16* __restrict__ Wu, const bf16* __restrict__ Wd,
                                       bf16* __restrict__ out, char* ws, char* lds) {
  const rsrc_t rs = __builtin_amdgcn_make_buffer_rsrc(ws, 0, WS_SPAN, 0x00020000);
  const bf16* xn = (const bf16*)(ws + OFF_XN);
  volatile int* tk = (volatile int*)(lds + 144 * 1024);
  for (;;) {
    __syncthreads();
    if (threadIdx.x == 0) *tk = (int)__hip_atomic_fetch_add(cnt_ptr(ws, C_TICKET), 1u, RLX_AGENT);
    __syncthreads();
    const int t = *tk;
    int tid = threadIdx.x; asm volatile("" : "+v"(tid));
    if (t >= T_END) break;
    if (t < T_KV) {
      fastkv_task(xn, Wk, Wv, lds, rs, t, tid);
      signal_cnt(ws, C_F);
    } else if (t < T_Q) {
      const int rb = (t - T_KV) >> 6, cb = t & 63;
      if (cb < 32) gemm_tile<0>(xn, DM, Wk, nullptr, NH * DH, DM, rb * 128, cb * 128, lds, rs, (unsigned)OFF_KF, tid);
      else gemm_tile<1>(xn, DM, Wv, nullptr, NH * DH, DM, rb * 128, (cb - 32) * 128, lds, rs, (unsigned)OFF_VF, tid);
      signal_cnt(ws, C_KV + rb);
    } else if (t < T_G) {
      const int u = t - T_Q, rb = u >> 5, cb = u & 31;
      gemm_tile<0>(xn, DM, Wq, nullptr, NH * DH, DM, rb * 128, cb * 128, lds, rs, (unsigned)OFF_QF, tid);
      signal_cnt(ws, C_Q + rb);
    } else if (t < T_W) {
      const int u = t - T_G, rb = u / 28, cb = u - rb * 28;
      gemm_tile<2>(xn, DM, Wg, Wu, FF, DM, rb * 128, cb * 64, lds, rs, (unsigned)OFF_H, tid);
      signal_cnt(ws, C_H + rb);
    } else if (t < T_B) {
      wof_task(Wo, lds, rs, t - T_W, tid);
      signal_cnt(ws, C_WOF);
    } else if (t < T_S) {
      const int u = t - T_B, rb = u >> 2, cb = u & 3;
      if (threadIdx.x == 0) wait_cnt(ws, C_H + rb, 28);
      acquire_all();
      gemm_tile<3>((const bf16*)(ws + OFF_H), FF, Wd, nullptr, DM, FF, rb * 128, cb * 128, lds, rs, (unsigned)OFF_BOUT, tid);
      signal_cnt(ws, C_BO + rb);
    } else {
      const int u = t - T_S, seg = u / 20, k = u - seg * 20;
      if (k < 4) xattn_task(ws, rs, seg, k * 8 + __builtin_amdgcn_readfirstlane(tid >> 6), tid);
      else final_task(ws, rs, lds, x, out, seg, k - 4, tid);
    }
  }
}

constexpr int CL_SCR = 0;
constexpr int CL_KX = 0;
constexpr int CL_VX = CL_KX + 8192;
constexpr int CL_MEM = CL_VX + 4096;
constexpr int CL_QF = CL_MEM + 8192;
constexpr int CL_KF = CL_QF + 4096;
constexpr int CL_OF = CL_KF + 4096;
constexpr int CL_P = CL_OF + 2048;
constexpr int CL_WV = 32768;
constexpr int CL_WO = 98304;
constexpr int CHAIN_LDS = 163840;
#define STAMP(k)
constexpr unsigned SENT = 0xFFFFFFFFu;
constexpr unsigned SPIN_CAP = 1u << 18;

__device__ __forceinline__ void chain(const bf16* __restrict__ Wq, const bf16* __restrict__ Wk, const bf16* __restrict__ Wv,
                                      const bf16* __restrict__ Wo, char* ws, char* lds) {
  const int tid = threadIdx.x, lane = tid & 63, w = __builtin_amdgcn_readfirstlane(tid >> 6);
  const int li = lane & 15, g = lane >> 4, q4 = li >> 2, p4 = li & 3;
  const int b = blockIdx.x, h = b >> 1, c = b & 1;
  bf16x8 wqk[2][16];
  {
    constexpr int P1 = 272, P2 = 1040, P3 = 144;
    u32x4 r[8];
    auto ld_qk = [&](int it) {
#pragma unroll
      for (int u = 0; u < 8; ++u) {
        const int cidx = tid + 512 * u, mat = cidx >> 11, rr = (cidx >> 4) & 127, ch = cidx & 15;
        r[u] = *(const u32x4*)((mat ? Wk : Wq) + (size_t)(128 * it + rr) * (NH * DH) + h * DH + ch * 8);
      }
    };
    auto ld_wo = [&]() {
#pragma unroll
      for (int u = 0; u < 8; ++u) {
        const int cidx = tid + 512 * u, rr = cidx >> 6, ch = cidx & 63;
        r[u] = *(const u32x4*)(Wo + ((size_t)h * DH + c * 64 + rr) * DM + ch * 8);
      }
    };
    auto ld_wv = [&]() {
#pragma unroll
      for (int u = 0; u < 8; ++u) {
        const int rr = 64 * u + (tid >> 3), ch = tid & 7;
        r[u] = *(const u32x4*)(Wv + (size_t)rr * (NH * DH) + h * DH + c * 64 + ch * 8);
      }
    };
    char* scr = lds + 32768;
    ld_qk(0);
#pragma unroll
    for (int it = 0; it < 4; ++it) {
#pragma unroll
      for (int u = 0; u < 8; ++u) {
        const int cidx = tid + 512 * u, mat = cidx >> 11, rr = (cidx >> 4) & 127, ch = cidx & 15;
        *(u32x4*)(scr + mat * (128 * P1) + rr * P1 + ch * 16) = r[u];
      }
      __syncthreads();
      if (it < 3) ld_qk(it + 1); else ld_wo();
      const char* base = scr + (w >> 2) * (128 * P1);
#pragma unroll
      for (int k4 = 0; k4 < 4; ++k4)
#pragma unroll
        for (int t = 0; t < 2; ++t) {
          const int c16 = 2 * (w & 3) + t;
          bf16x4 lo = tr_read(base, (32 * k4 + 8 * g + q4) * P1 + (16 * c16 + 4 * p4) * 2);
          bf16x4 hi = tr_read(base, (32 * k4 + 8 * g + 4 + q4) * P1 + (16 * c16 + 4 * p4) * 2);
          wqk[t][4 * it + k4] = cat(lo, hi);
        }
      __syncthreads();
    }
    scr = lds;
#pragma unroll
    for (int u = 0; u < 8; ++u) {
      const int cidx = tid + 512 * u, rr = cidx >> 6, ch = cidx & 63;
      *(u32x4*)(scr + rr * P2 + ch * 16) = r[u];
    }
    __syncthreads();
    ld_wv();
#pragma unroll
    for (int nt = 0; nt < 4; ++nt)
#pragma unroll
      for (int k2 = 0; k2 < 2; ++k2) {
        const int col = 64 * w + 16 * nt + 4 * p4;
        bf16x4 lo = tr_read(scr, ((2 * k2 + 0) * 16 + 4 * g + q4) * P2 + col * 2);
        bf16x4 hi = tr_read(scr, ((2 * k2 + 1) * 16 + 4 * g + q4) * P2 + col * 2);
        *(bf16x8*)(lds + CL_WO + (((w * 4 + nt) * 2 + k2) * 64 + lane) * 16) = cat(lo, hi);
      }
    __syncthreads();
#pragma unroll
    for (int it = 0; it < 4; ++it) {
#pragma unroll
      for (int u = 0; u < 2; ++u) *(u32x4*)(scr + (64 * u + (tid >> 3)) * P3 + (tid & 7) * 16) = r[2 * it + u];
      __syncthreads();
#pragma unroll
      for (int e2 = 0; e2 < 2; ++e2) {
        const int et = 2 * (w & 1) + e2, k4 = w >> 1;
        bf16x4 lo = tr_read(scr, (32 * k4 + 8 * g + q4) * P3 + (16 * et + 4 * p4) * 2);
        bf16x4 hi = tr_read(scr, (32 * k4 + 8 * g + 4 + q4) * P3 + (16 * et + 4 * p4) * 2);
        *(bf16x8*)(lds + CL_WV + ((et * 16 + 4 * it + k4) * 64 + lane) * 16) = cat(lo, hi);
      }
      __syncthreads();
    }
  }
  const auto rs_mem = __builtin_amdgcn_make_buffer_rsrc(ws + OFF_MEMH, 0, 65 * 8192, 0x00020000);
  const auto rs_part = __builtin_amdgcn_make_buffer_rsrc(ws + OFF_PART, 0, 1 << 20, 0x00020000);
  const auto rs_km = __builtin_amdgcn_make_buffer_rsrc(ws + OFF_KMH, 0, 16 << 20, 0x00020000);
  const float scale = 0.08838834764831845f;
  const auto rs_kv = __builtin_amdgcn_make_buffer_rsrc(ws + OFF_KF, 0, 32 << 20, 0x00020000);
  unsigned rdy = 0;
  if (w < 4) {
    unsigned spins = 0;
    while (__builtin_amdgcn_readfirstlane(__hip_atomic_load(cnt_ptr(ws, C_F), RLX_AGENT)) < 128u) { __builtin_amdgcn_s_sleep(4); if (++spins > SPIN_CAP) break; }
    const u32x4 a0 = __builtin_amdgcn_raw_buffer_load_b128(rs_kv, lane * 16, h * 8192 + w * 2048, 16);
    const u32x4 a1 = __builtin_amdgcn_raw_buffer_load_b128(rs_kv, lane * 16, h * 8192 + w * 2048 + 1024, 16);
    const u32x4 a2 = __builtin_amdgcn_raw_buffer_load_b128(rs_kv, lane * 16, (16 << 20) + h * 8192 + c * 4096 + w * 1024, 16);
    *(u32x4*)(lds + lane * 16 + CL_KX + w * 2048) = a0;
    *(u32x4*)(lds + lane * 16 + CL_KX + w * 2048 + 1024) = a1;
    *(u32x4*)(lds + lane * 16 + CL_VX + w * 1024) = a2;
    rdy = 0u;
  }
  const int pst_off = (((8 * w + 2 * (g & 1) + (g >> 1)) * 64 + b) * 8 + li) * 16;
  const char* l16 = lds + lane * 16;
  const char* mfb = lds + CL_MEM + (g * 8 + (li & 7)) * 16;
  const char* wvb = lds + CL_WV + ((w & 3) * 1024 + lane) * 16;
  const char* wob = lds + CL_WO + (w * 512 + lane) * 16;
  for (int s = 0; s < NSEG; ++s) {
    if (w >= 6) {
      u32x4 v[4]; unsigned spins = 0;
      asm volatile("s_waitcnt vmcnt(0)" ::: "memory");
      for (;;) {
        asm volatile("" ::: "memory");
#pragma unroll
        for (int u = 0; u < 4; ++u) v[u] = __builtin_amdgcn_raw_buffer_load_b128(rs_mem, lane * 16, s * 8192 + (w - 6) * 4096 + u * 1024, 16);
        unsigned m = 0;
#pragma unroll
        for (int u = 0; u < 4; ++u) m = max(m, max(v[u][0], v[u][2]));
        if (__all(m != SENT)) break;
        if (++spins > SPIN_CAP) break;
      }
#pragma unroll
      for (int u = 0; u < 4; ++u) *(u32x4*)(l16 + CL_MEM + (w - 6) * 4096 + u * 1024) = v[u];
    }
    STAMP(0);
    __syncthreads();
    STAMP(1);
    if (tid == 256 && s > 0) __hip_atomic_store(cnt_ptr(ws, C_CH + s - 1) + b, 1u, RLX_AGENT);
    u32x4 pf[3];
    const int sn = (s + 1 < NSEG) ? s + 1 : s;
    const int so = (sn * NH + h) * 8192;
    bool early = true;
    if (w < 4) {
      early = __builtin_amdgcn_readfirstlane(rdy) >= 64u;
      if (early) {
        pf[0] = __builtin_amdgcn_raw_buffer_load_b128(rs_kv, lane * 16, so + w * 2048, 16);
        pf[1] = __builtin_amdgcn_raw_buffer_load_b128(rs_kv, lane * 16, so + w * 2048 + 1024, 16);
        pf[2] = __builtin_amdgcn_raw_buffer_load_b128(rs_kv, lane * 16, (16 << 20) + so + c * 4096 + w * 1024, 16);
      }
      const int s2 = (s + 2 < NSEG) ? s + 2 : NSEG - 1;
      rdy = __hip_atomic_load(cnt_ptr(ws, C_KV + (s2 >> 2)), RLX_AGENT);
    }
    bf16x8 mf[16];
#pragma unroll
    for (int ks = 0; ks < 16; ++ks) mf[ks] = *(const bf16x8*)(mfb + ks * 512);
    __builtin_amdgcn_sched_barrier(0);
    {
      f32x4 a0 = {0, 0, 0, 0}, a1 = {0, 0, 0, 0};
#pragma unroll
      for (int ks = 0; ks < 16; ++ks) {
        a0 = mfma32(wqk[0][ks], mf[ks], a0);
        a1 = mfma32(wqk[1][ks], mf[ks], a1);
      }
      const bf16x8 o = cat(pk4(a0), pk4(a1));
      *(bf16x8*)(lds + (w < 4 ? CL_QF : CL_KF) + ((w & 3) * 64 + lane) * 16) = o;
      if (w >= 4 && c == 0) __builtin_amdgcn_raw_buffer_store_b128(__builtin_bit_cast(u32x4, o), rs_km, lane * 16, ((s * NH + h) * 4 + (w & 3)) * 1024, 16);
    }
    STAMP(2);
    bf16x4 vm4 = {(__bf16)0.f, (__bf16)0.f, (__bf16)0.f, (__bf16)0.f};
    if (w == 0) {
      bf16x8 kxf[8];
#pragma unroll
      for (int u = 0; u < 8; ++u) kxf[u] = *(const bf16x8*)(l16 + CL_KX + u * 1024);
      __syncthreads();
      f32x4 s0 = {0, 0, 0, 0}, s1 = {0, 0, 0, 0}, s2 = {0, 0, 0, 0};
#pragma unroll
      for (int kk = 0; kk < 4; ++kk) {
        const bf16x8 qf = *(const bf16x8*)(l16 + CL_QF + kk * 1024);
        const bf16x8 k0 = *(const bf16x8*)(l16 + CL_KF + kk * 1024);
        s0 = mfma32(k0, qf, s0); s1 = mfma32(kxf[kk], qf, s1); s2 = mfma32(kxf[4 + kk], qf, s2);
      }
      if (g >= 2) s0 = (f32x4){-3.0e38f, -3.0e38f, -3.0e38f, -3.0e38f};
      float m = fmaxf(fmaxf(fmaxf(s0[0], s0[1]), fmaxf(s0[2], s0[3])), fmaxf(fmaxf(fmaxf(s1[0], s1[1]), fmaxf(s1[2], s1[3])), fmaxf(fmaxf(s2[0], s2[1]), fmaxf(s2[2], s2[3]))));
      m = xmax16_32(m);
      const float cexp = scale * 1.4426950408889634f, mc = -m * cexp;
      bf16x4 p0; bf16x8 p12;
#pragma unroll
      for (int r = 0; r < 4; ++r) {
        p0[r] = (__bf16)__builtin_amdgcn_exp2f(fmaf(s0[r], cexp, mc));
        p12[r] = (__bf16)__builtin_amdgcn_exp2f(fmaf(s1[r], cexp, mc));
        p12[4 + r] = (__bf16)__builtin_amdgcn_exp2f(fmaf(s2[r], cexp, mc));
      }
      *(bf16x8*)(lds + CL_P + lane * 16) = p12;
      *(bf16x4*)(lds + CL_P + 1024 + lane * 8) = p0;
    } else {
      if (w >= 4) {
        f32x4 va = {0, 0, 0, 0}, vb = {0, 0, 0, 0};
        bf16x8 wv[2][4];
#pragma unroll
        for (int k1 = 0; k1 < 4; ++k1) wv[0][k1] = *(const bf16x8*)(wvb + k1 * 1024);
        __syncthreads();
#pragma unroll
        for (int k4 = 0; k4 < 4; ++k4) {
          if (k4 < 3) {
#pragma unroll
            for (int k1 = 0; k1 < 4; ++k1) wv[(k4 + 1) & 1][k1] = *(const bf16x8*)(wvb + (4 * k4 + 4 + k1) * 1024);
          }
#pragma unroll
          for (int k1 = 0; k1 < 4; k1 += 2) { va = mfma32(mf[4 * k4 + k1], wv[k4 & 1][k1], va); vb = mfma32(mf[4 * k4 + k1 + 1], wv[k4 & 1][k1 + 1], vb); }
          __builtin_amdgcn_sched_barrier(0);
        }
        vm4 = pk4(va + vb);
        __builtin_amdgcn_raw_buffer_store_b64(__builtin_bit_cast(u32x2, vm4), rs_km, lane * 8, (8 << 20) + ((s * NH + h) * 8 + 4 * c + (w - 4)) * 512, 16);
      } else {
        __syncthreads();
      }
    }
    bf16x8 wo[4][2];
#pragma unroll
    for (int nt = 0; nt < 4; ++nt)
#pragma unroll
      for (int k2 = 0; k2 < 2; ++k2) wo[nt][k2] = *(const bf16x8*)(wob + (nt * 2 + k2) * 1024);
    __syncthreads();
    if (w >= 4) {
      const bf16x8 p12 = *(const bf16x8*)(lds + CL_P + lane * 16);
      const bf16x4 p0 = *(const bf16x4*)(lds + CL_P + 1024 + lane * 8);
      const bf16x8 vx8 = *(const bf16x8*)(lds + CL_VX + ((w - 4) * 64 + lane) * 16);
      f32x4 o = mfma16(vm4, p0, (f32x4){0, 0, 0, 0});
      const bf16x4 one4 = {(__bf16)1.f, (__bf16)1.f, (__bf16)1.f, (__bf16)1.f};
      f32x4 l = mfma16(one4, p0, (f32x4){0, 0, 0, 0});
      o = mfma32(vx8, p12, o);
      l = mfma32(cat(one4, one4), p12, l);
      const float inv = __builtin_amdgcn_rcpf(l[0]);
      *(bf16x4*)(lds + CL_OF + (((w - 4) >> 1) * 64 + lane) * 16 + (w & 1) * 8) = pk4(o * inv);
    }
    STAMP(3);
    __syncthreads();
    STAMP(4);
    if (s + 1 == NSEG) {
      if (w >= 4) asm volatile("s_waitcnt vmcnt(0)" ::: "memory");
      __syncthreads();
      if (tid == 64) __hip_atomic_store(cnt_ptr(ws, C_CH + s) + b, 1u, RLX_AGENT);
      break;
    }
    const int slot = (s & 1) << 19;
    {
      const bf16x8 of0 = *(const bf16x8*)(l16 + CL_OF);
      const bf16x8 of1 = *(const bf16x8*)(l16 + CL_OF + 1024);
#pragma unroll
      for (int n2 = 0; n2 < 2; ++n2) {
        f32x4 pa = mfma32(wo[2 * n2][0], of0, (f32x4){0, 0, 0, 0});
        f32x4 pb = mfma32(wo[2 * n2 + 1][0], of0, (f32x4){0, 0, 0, 0});
        pa = mfma32(wo[2 * n2][1], of1, pa);
        pb = mfma32(wo[2 * n2 + 1][1], of1, pb);
        u32x2 A = __builtin_bit_cast(u32x2, pk4(pa)), B = __builtin_bit_cast(u32x2, pk4(pb));
        auto r0 = __builtin_amdgcn_permlane16_swap(A[0], B[0], false, false);
        auto r1 = __builtin_amdgcn_permlane16_swap(A[1], B[1], false, false);
        u32x4 pv = {r0[0] | 1u, r1[0], r0[1] | 1u, r1[1]};
        if (li < 8) __builtin_amdgcn_raw_buffer_store_b128(pv, rs_part, pst_off, slot + n2 * 32768, 16);
      }
    }
    STAMP(5);
    if (w < 4) {
      if (!early) {
        unsigned spins = 0;
        while (__builtin_amdgcn_readfirstlane(__hip_atomic_load(cnt_ptr(ws, C_KV + (sn >> 2)), RLX_AGENT)) < 64u) { __builtin_amdgcn_s_sleep(2); if (++spins > SPIN_CAP) break; }
        pf[0] = __builtin_amdgcn_raw_buffer_load_b128(rs_kv, lane * 16, so + w * 2048, 16);
        pf[1] = __builtin_amdgcn_raw_buffer_load_b128(rs_kv, lane * 16, so + w * 2048 + 1024, 16);
        pf[2] = __builtin_amdgcn_raw_buffer_load_b128(rs_kv, lane * 16, (16 << 20) + so + c * 4096 + w * 1024, 16);
      }
      *(u32x4*)(l16 + CL_KX + w * 2048) = pf[0];
      *(u32x4*)(l16 + CL_KX + w * 2048 + 1024) = pf[1];
      *(u32x4*)(l16 + CL_VX + w * 1024) = pf[2];
    }
    if (w == 4 || w == 5) asm volatile("s_waitcnt vmcnt(0)" ::: "memory");
    if (w >= 6) {
      u32x2 v[8]; unsigned spins = 0;
      const int roff = lane * 16 + (w - 6) * 8;
      asm volatile("s_waitcnt vmcnt(0)" ::: "memory");
      for (;;) {
        asm volatile("" ::: "memory");
#pragma unroll
        for (int it = 0; it < 8; ++it) v[it] = __builtin_amdgcn_raw_buffer_load_b64(rs_part, roff, slot + b * 8192 + it * 1024, 16);
        unsigned m = 1u;
#pragma unroll
        for (int it = 0; it < 8; ++it) m &= v[it][0];
        if (__all(m != 0u)) break;
        if (++spins > SPIN_CAP) break;
      }
      STAMP(6);
      f32x4 a = {0, 0, 0, 0};
#pragma unroll
      for (int it = 0; it < 8; ++it) {
        a[0] += __builtin_bit_cast(float, v[it][0] << 16); a[1] += __builtin_bit_cast(float, v[it][0] & 0xffff0000u);
        a[2] += __builtin_bit_cast(float, v[it][1] << 16); a[3] += __builtin_bit_cast(float, v[it][1] & 0xffff0000u);
      }
#pragma unroll
      for (int r = 0; r < 4; ++r) a[r] = xsum8_16_32(a[r]);
      u32x2 o = __builtin_bit_cast(u32x2, pk4(a));
      if (o[0] == SENT) o[0] = 0x7FFF7FFFu;
      if (lane < 8) __builtin_amdgcn_raw_buffer_store_b64(o, rs_mem, lane * 16 + (w - 6) * 8, (s + 1) * 8192 + b * 128, 16);
      const u32x2 z = {0u, 0u};
#pragma unroll
      for (int it = 0; it < 8; ++it) __builtin_amdgcn_raw_buffer_store_b64(z, rs_part, roff, slot + b * 8192 + it * 1024, 16);
      STAMP(7);
    }
  }
}

__global__ __launch_bounds__(512, 2) void k_main(const bf16* __restrict__ x, const bf16* __restrict__ Wq, const bf16* __restrict__ Wk,
                                                 const bf16* __restrict__ Wv, const bf16* __restrict__ Wo, const bf16* __restrict__ Wg,
                                                 const bf16* __restrict__ Wu, const bf16* __restrict__ Wd, bf16* __restrict__ out, char* ws) {
  extern __shared__ __attribute__((aligned(16))) char lds[];
  if (blockIdx.x < 64) chain(Wq, Wk, Wv, Wo, ws, lds);
  else worker(x, Wq, Wk, Wv, Wo, Wg, Wu, Wd, out, ws, lds);
}

extern "C" void kernel_launch(void* const* d_in, const int* in_sizes, int n_in,
                              void* d_out, int out_size, void* d_ws, size_t ws_size,
                              hipStream_t stream) {
  const bf16* x = (const bf16*)d_in[0];
  const bf16* mem0 = (const bf16*)d_in[1];
  const bf16* Wq = (const bf16*)d_in[2];
  const bf16* Wk = (const bf16*)d_in[3];
  const bf16* Wv = (const bf16*)d_in[4];
  const bf16* Wo = (const bf16*)d_in[5];
  const bf16* Wg = (const bf16*)d_in[8];
  const bf16* Wu = (const bf16*)d_in[9];
  const bf16* Wd = (const bf16*)d_in[10];
  char* ws = (char*)d_ws;
  (void)hipFuncSetAttribute((const void*)k_main, hipFuncAttributeMaxDynamicSharedMemorySize, CHAIN_LDS);
  k_ln<<<S_ / 4, 256, 0, stream>>>(x, mem0, ws);
  k_main<<<256, 512, CHAIN_LDS, stream>>>(x, Wq, Wk, Wv, Wo, Wg, Wu, Wd, (bf16*)d_out, ws);
}
```

```cpp
#include <hip/hip_runtime.h>
#include <hip/hip_bf16.h>

constexpr int S_ = 2048, DM = 512, NH = 32, DH = 128, MM = 8, SEG = 32, NSEG = 64, FF = 1792;

typedef __bf16 bf16;
typedef __bf16 bf16x8 __attribute__((ext_vector_type(8)));
typedef __bf16 bf16x4 __attribute__((ext_vector_type(4)));
typedef short s16x4 __attribute__((ext_vector_type(4)));
typedef float f32x4 __attribute__((ext_vector_type(4)));
typedef unsigned u32x4 __attribute__((ext_vector_type(4)));
typedef unsigned u32x2 __attribute__((ext_vector_type(2)));
#define LAS __attribute__((address_space(3)))

constexpr size_t OFF_MEMH = 0;
constexpr size_t OFF_PART = 1 << 20;
constexpr size_t OFF_CNT  = 2 << 20;
constexpr size_t OFF_XN   = 4u << 20;
constexpr size_t OFF_BOUT = 6u << 20;
constexpr size_t OFF_H    = 8u << 20;
constexpr size_t OFF_QF   = 16u << 20;
constexpr size_t OFF_KF   = 32u << 20;
constexpr size_t OFF_VF   = 48u << 20;
constexpr size_t OFF_OX   = 64u << 20;
constexpr size_t OFF_KMH  = 80u << 20;
constexpr size_t OFF_VMH  = 88u << 20;
constexpr size_t OFF_WOF  = 96u << 20;
constexpr unsigned WS_SPAN = 100u << 20;
constexpr int C_TICKET = 0, C_KV = 1, C_Q = 17, C_H = 33, C_BO = 49, C_WOF = 65, C_X = 66, C_CH = 130, C_F = 194;
#define RLX_AGENT __ATOMIC_RELAXED, __HIP_MEMORY_SCOPE_AGENT

__device__ __forceinline__ f32x4 mfma32(bf16x8 a, bf16x8 b, f32x4 c) {
  return __builtin_amdgcn_mfma_f32_16x16x32_bf16(a, b, c, 0, 0, 0);
}
__device__ __forceinline__ f32x4 mfma16(bf16x4 a, bf16x4 b, f32x4 c) {
  bf16x8 a8, b8;
#pragma unroll
  for (int j = 0; j < 4; ++j) { a8[j] = a[j]; b8[j] = b[j]; a8[4 + j] = (__bf16)0.f; b8[4 + j] = (__bf16)0.f; }
  return __builtin_amdgcn_mfma_f32_16x16x32_bf16(a8, b8, c, 0, 0, 0);
}
__device__ __forceinline__ float bfr(float x) { return (float)(__bf16)x; }
__device__ __forceinline__ bf16x4 tr_read(const char* lds_base, unsigned off) {
  s16x4 t = __builtin_amdgcn_ds_read_tr16_b64_v4i16((LAS s16x4*)(lds_base + off));
  return __builtin_bit_cast(bf16x4, t);
}
__device__ __forceinline__ bf16x8 cat(bf16x4 a, bf16x4 b) {
  bf16x8 r; r[0] = a[0]; r[1] = a[1]; r[2] = a[2]; r[3] = a[3]; r[4] = b[0]; r[5] = b[1]; r[6] = b[2]; r[7] = b[3]; return r;
}
__device__ __forceinline__ bf16x4 pk4(f32x4 v) { bf16x4 r; r[0] = (__bf16)v[0]; r[1] = (__bf16)v[1]; r[2] = (__bf16)v[2]; r[3] = (__bf16)v[3]; return r; }

__device__ __forceinline__ float xmax16_32(float x) {
  unsigned u = __builtin_bit_cast(unsigned, x);
  auto a = __builtin_amdgcn_permlane16_swap(u, u, false, false);
  x = fmaxf(__builtin_bit_cast(float, (unsigned)a[0]), __builtin_bit_cast(float, (unsigned)a[1]));
  u = __builtin_bit_cast(unsigned, x);
  auto c = __builtin_amdgcn_permlane32_swap(u, u, false, false);
  return fmaxf(__builtin_bit_cast(float, (unsigned)c[0]), __builtin_bit_cast(float, (unsigned)c[1]));
}
__device__ __forceinline__ float xsum8_16_32(float x) {
  x += __builtin_bit_cast(float, __builtin_amdgcn_update_dpp(0, __builtin_bit_cast(int, x), 0x128, 0xf, 0xf, false));
  unsigned u = __builtin_bit_cast(unsigned, x);
  auto a = __builtin_amdgcn_permlane16_swap(u, u, false, false);
  x = __builtin_bit_cast(float, (unsigned)a[0]) + __builtin_bit_cast(float, (unsigned)a[1]);
  u = __builtin_bit_cast(unsigned, x);
  auto c = __builtin_amdgcn_permlane32_swap(u, u, false, false);
  return __builtin_bit_cast(float, (unsigned)c[0]) + __builtin_bit_cast(float, (unsigned)c[1]);
}

__global__ __launch_bounds__(256) void k_ln(const bf16* __restrict__ x, const bf16* __restrict__ mem0, char* ws) {
  const int lane = threadIdx.x & 63, row = blockIdx.x * 4 + (threadIdx.x >> 6);
  bf16* xn = (bf16*)(ws + OFF_XN);
  bf16x8 v = *(const bf16x8*)(x + (size_t)row * DM + lane * 8);
  float f[8], s = 0.f;
#pragma unroll
  for (int j = 0; j < 8; ++j) { f[j] = (float)v[j]; s += f[j]; }
#pragma unroll
  for (int m = 32; m >= 1; m >>= 1) s += __shfl_xor(s, m);
  const float mean = s * (1.f / DM);
  float q = 0.f;
#pragma unroll
  for (int j = 0; j < 8; ++j) { float d = f[j] - mean; q += d * d; }
#pragma unroll
  for (int m = 32; m >= 1; m >>= 1) q += __shfl_xor(q, m);
  const float mb = bfr(mean), vb = bfr(q * (1.f / DM));
  const float den = bfr(sqrtf(bfr(vb + 1e-5f)));
  bf16x8 o;
#pragma unroll
  for (int j = 0; j < 8; ++j) o[j] = (__bf16)(bfr(f[j] - mb) / den);
  *(bf16x8*)(xn + (size_t)row * DM + lane * 8) = o;
  {
    const u32x4 ff = {0xFFFFFFFFu, 0xFFFFFFFFu, 0xFFFFFFFFu, 0xFFFFFFFFu}, zz = {0u, 0u, 0u, 0u};
    if (threadIdx.x < 64) *(u32x4*)(ws + OFF_MEMH + 8192 + ((size_t)blockIdx.x * 64 + threadIdx.x) * 16) = ff;
    if (threadIdx.x < 136) *(u32x4*)(ws + OFF_PART + ((size_t)blockIdx.x * 136 + threadIdx.x) * 16) = zz;
  }
  if (blockIdx.x == 0) {
    for (int c = threadIdx.x; c < 512; c += 256) {
      const int i = c & 7, dg = c >> 3;
      *(bf16x8*)(ws + OFF_MEMH + (size_t)c * 16) = *(const bf16x8*)(mem0 + i * DM + dg * 8);
    }
  }
}

constexpr int GA_PITCH = 272;
constexpr int GA_BYTES = 128 * GA_PITCH;
constexpr int GB_BYTES = 128 * 256;
__device__ __forceinline__ unsigned off_b(unsigned row, unsigned ch) { return 256u * row + 16u * (ch ^ (((row & 3) << 2) | ((row >> 2) & 3))); }
typedef __amdgpu_buffer_rsrc_t rsrc_t;
__device__ __forceinline__ void st16(rsrc_t rs, unsigned off, bf16x8 v) { __builtin_amdgcn_raw_buffer_store_b128(__builtin_bit_cast(u32x4, v), rs, off, 0, 16); }
__device__ __forceinline__ void st8(rsrc_t rs, unsigned off, bf16x4 v) { __builtin_amdgcn_raw_buffer_store_b64(__builtin_bit_cast(u32x2, v), rs, off, 0, 16); }

__device__ __forceinline__ bf16x8 ld16(rsrc_t rs, unsigned voff, unsigned soff) { return __builtin_bit_cast(bf16x8, __builtin_amdgcn_raw_buffer_load_b128(rs, voff, soff, 16)); }
__device__ __forceinline__ bf16x4 ld8(rsrc_t rs, unsigned voff, unsigned soff) { return __builtin_bit_cast(bf16x4, __builtin_amdgcn_raw_buffer_load_b64(rs, voff, soff, 16)); }

template <int MODE>
__device__ __forceinline__ void gemm_tile(const bf16* __restrict__ A, int lda, const bf16* __restrict__ B0, const bf16* __restrict__ B1,
                                          int ldb, int K, int row0, int col0, char* lds, rsrc_t rs, unsigned dst, int tid) {
  const int lane = tid & 63, w = tid >> 6, wm = w >> 1, wn = w & 1;
  const int li = lane & 15, g = lane >> 4, q = li >> 2, p = li & 3;
  char* la = lds; char* lb = lds + GA_BYTES;
  f32x4 acc[2][4];
#pragma unroll
  for (int a = 0; a < 2; ++a)
#pragma unroll
    for (int b = 0; b < 4; ++b) acc[a][b] = (f32x4){0.f, 0.f, 0.f, 0.f};
  const bf16* asrc[4]; const bf16* bsrc[4]; unsigned adst[4], bdst[4];
#pragma unroll
  for (int u = 0; u < 4; ++u) {
    const int c = tid + 512 * u;
    asrc[u] = A + (size_t)(row0 + (c >> 4)) * lda + (c & 15) * 8;
    adst[u] = (c >> 4) * GA_PITCH + (c & 15) * 16;
    const int kr = c >> 4, ch = c & 15;
    if (MODE == 2) {
      const int blk = ch >> 2;
      bsrc[u] = ((blk & 1) ? B1 : B0) + (size_t)kr * ldb + col0 + (blk >> 1) * 32 + (ch & 3) * 8;
    } else {
      bsrc[u] = B0 + (size_t)kr * ldb + col0 + ch * 8;
    }
    bdst[u] = off_b(kr, ch);
  }
  auto compute = [&]() {
#pragma unroll
    for (int ks = 0; ks < 4; ++ks) {
      bf16x8 af[2], bfr_[4];
#pragma unroll
      for (int tt = 0; tt < 2; ++tt) af[tt] = *(const bf16x8*)(la + (wm * 32 + tt * 16 + li) * GA_PITCH + ks * 64 + g * 16);
#pragma unroll
      for (int nt = 0; nt < 4; ++nt) {
        const unsigned ch = 2 * (wn * 4 + nt) + (p >> 1);
        bf16x4 lo = tr_read(lb, off_b(32 * ks + 8 * g + q, ch) + 8 * (p & 1));
        bf16x4 hi = tr_read(lb, off_b(32 * ks + 8 * g + 4 + q, ch) + 8 * (p & 1));
        bfr_[nt] = cat(lo, hi);
      }
#pragma unroll
      for (int tt = 0; tt < 2; ++tt)
#pragma unroll
        for (int nt = 0; nt < 4; ++nt) {
          if (MODE == 1) acc[tt][nt] = mfma32(af[tt], bfr_[nt], acc[tt][nt]);
          else acc[tt][nt] = mfma32(bfr_[nt], af[tt], acc[tt][nt]);
        }
    }
  };
  {
    u32x4 ra[4], rb[4];
#pragma unroll
    for (int u = 0; u < 4; ++u) { ra[u] = *(const u32x4*)asrc[u]; rb[u] = *(const u32x4*)bsrc[u]; }
    const int nkt = K >> 7;
    for (int kt = 0; kt < nkt; ++kt) {
#pragma unroll
      for (int u = 0; u < 4; ++u) { *(u32x4*)(la + adst[u]) = ra[u]; *(u32x4*)(lb + bdst[u]) = rb[u]; }
      __syncthreads();
      if (kt + 1 < nkt) {
#pragma unroll
        for (int u = 0; u < 4; ++u) {
          ra[u] = *(const u32x4*)(asrc[u] + (kt + 1) * 128);
          rb[u] = *(const u32x4*)(bsrc[u] + (size_t)(kt + 1) * 128 * ldb);
        }
      }
      compute();
      __syncthreads();
    }
  }
  if (MODE == 0) {
    const int seg = (row0 >> 5) + wm, head = col0 >> 7;
#pragma unroll
    for (int tt = 0; tt < 2; ++tt)
#pragma unroll
      for (int k2 = 0; k2 < 2; ++k2)
        st16(rs, dst + ((((seg * NH + head) * 2 + tt) * 4 + (wn * 2 + k2)) * 64 + lane) * 16, cat(pk4(acc[tt][2 * k2]), pk4(acc[tt][2 * k2 + 1])));
  } else if (MODE == 1) {
    const int seg = (row0 >> 5) + wm, head = col0 >> 7;
#pragma unroll
    for (int nt = 0; nt < 4; ++nt)
      st16(rs, dst + (((seg * NH + head) * 8 + (wn * 4 + nt)) * 64 + lane) * 16, cat(pk4(acc[0][nt]), pk4(acc[1][nt])));
  } else if (MODE == 2) {
#pragma unroll
    for (int tt = 0; tt < 2; ++tt)
#pragma unroll
      for (int n2 = 0; n2 < 2; ++n2) {
        f32x4 hv;
#pragma unroll
        for (int r = 0; r < 4; ++r) {
          const float gg = bfr(acc[tt][n2][r]), uu = bfr(acc[tt][n2 + 2][r]);
          const float sg = bfr(1.f / (1.f + __expf(-gg)));
          hv[r] = sg * uu;
        }
        const int row = row0 + wm * 32 + tt * 16 + li, col = col0 + 32 * wn + 16 * n2 + 4 * g;
        st8(rs, dst + (row * FF + col) * 2, pk4(hv));
      }
  } else {
#pragma unroll
    for (int tt = 0; tt < 2; ++tt)
#pragma unroll
      for (int nt = 0; nt < 4; ++nt) {
        const int row = row0 + wm * 32 + tt * 16 + li, col = col0 + 64 * wn + 16 * nt + 4 * g;
        st8(rs, dst + (row * DM + col) * 2, pk4(acc[tt][nt]));
      }
  }
}

__device__ __forceinline__ unsigned* cnt_ptr(char* ws, int idx) { return (unsigned*)(ws + OFF_CNT) + idx * 64; }
__device__ __forceinline__ void wait_cnt(char* ws, int idx, unsigned target) {
  unsigned spins = 0;
  while (__hip_atomic_load(cnt_ptr(ws, idx), RLX_AGENT) < target) { __builtin_amdgcn_s_sleep(4); if (++spins > (1u << 18)) break; }
}
__device__ __forceinline__ void acquire_all() {
  if (threadIdx.x == 0) { __builtin_amdgcn_fence(__ATOMIC_ACQUIRE, "agent"); asm volatile("s_waitcnt vmcnt(0)" ::: "memory"); }
  __syncthreads();
}
__device__ __forceinline__ void signal_cnt(char* ws, int idx) {
  asm volatile("s_waitcnt vmcnt(0)" ::: "memory");
  __syncthreads();
  if (threadIdx.x == 0) __hip_atomic_fetch_add(cnt_ptr(ws, idx), 1u, RLX_AGENT);
}

__device__ __forceinline__ void xattn_task(char* ws, rsrc_t rs, int seg, int head, int tid) {
  const int lane = tid & 63, li = lane & 15, g = lane >> 4;
  const int unit = seg * NH + head, rb = seg >> 2;
  const unsigned qf = (unsigned)OFF_QF + unit * 8192, kf = (unsigned)OFF_KF + unit * 8192, vf = (unsigned)OFF_VF + unit * 8192;
  const unsigned km = (unsigned)OFF_KMH + unit * 4096, vm = (unsigned)OFF_VMH + unit * 4096;
  const float scale = 0.08838834764831845f;
  if (threadIdx.x == 0) { wait_cnt(ws, C_Q + rb, 32); wait_cnt(ws, C_KV + rb, 64); }
  __syncthreads();
  f32x4 sc[3][2];
#pragma unroll
  for (int a = 0; a < 3; ++a) { sc[a][0] = (f32x4){0, 0, 0, 0}; sc[a][1] = (f32x4){0, 0, 0, 0}; }
  bf16x8 q[2][4], vx[8];
#pragma unroll
  for (int kk = 0; kk < 4; ++kk) {
    q[0][kk] = ld16(rs, lane * 16, qf + kk * 1024); q[1][kk] = ld16(rs, lane * 16, qf + 4096 + kk * 1024);
    const bf16x8 k1 = ld16(rs, lane * 16, kf + kk * 1024), k2 = ld16(rs, lane * 16, kf + 4096 + kk * 1024);
    sc[1][0] = mfma32(k1, q[0][kk], sc[1][0]); sc[1][1] = mfma32(k1, q[1][kk], sc[1][1]);
    sc[2][0] = mfma32(k2, q[0][kk], sc[2][0]); sc[2][1] = mfma32(k2, q[1][kk], sc[2][1]);
  }
#pragma unroll
  for (int et = 0; et < 8; ++et) vx[et] = ld16(rs, lane * 16, vf + et * 1024);
  if (threadIdx.x < 64) {
    unsigned spins = 0;
    while (!__all(__hip_atomic_load(cnt_ptr(ws, C_CH + seg) + threadIdx.x, RLX_AGENT) != 0u)) { __builtin_amdgcn_s_sleep(4); if (++spins > (1u << 18)) break; }
  }
  __syncthreads();
  bf16x8 k0[4]; bf16x4 vmf[8];
#pragma unroll
  for (int kk = 0; kk < 4; ++kk) k0[kk] = ld16(rs, lane * 16, km + kk * 1024);
#pragma unroll
  for (int et = 0; et < 8; ++et) vmf[et] = ld8(rs, lane * 8, vm + et * 512);
#pragma unroll
  for (int kk = 0; kk < 4; ++kk) { sc[0][0] = mfma32(k0[kk], q[0][kk], sc[0][0]); sc[0][1] = mfma32(k0[kk], q[1][kk], sc[0][1]); }
  bf16x4 p0[2]; bf16x8 p12[2];
#pragma unroll
  for (int qt = 0; qt < 2; ++qt) {
    const int qi = 16 * qt + li;
    float v[12]; float m = -3.0e38f;
#pragma unroll
    for (int kt = 0; kt < 3; ++kt)
#pragma unroll
      for (int r = 0; r < 4; ++r) {
        float s = bfr(bfr(sc[kt][qt][r]) * scale);
        bool ok = (kt == 0) ? (g < 2) : (16 * (kt - 1) + 4 * g + r <= qi);
        s = ok ? s : -3.0e38f;
        v[kt * 4 + r] = s; m = fmaxf(m, s);
      }
    m = xmax16_32(m);
    float sum = 0.f;
#pragma unroll
    for (int e = 0; e < 12; ++e) { v[e] = (v[e] > -1.0e38f) ? __expf(v[e] - m) : 0.f; sum += v[e]; }
    sum += __shfl_xor(sum, 16); sum += __shfl_xor(sum, 32);
    const float inv = 1.f / sum;
#pragma unroll
    for (int r = 0; r < 4; ++r) { p0[qt][r] = (__bf16)(v[r] * inv); p12[qt][r] = (__bf16)(v[4 + r] * inv); p12[qt][4 + r] = (__bf16)(v[8 + r] * inv); }
  }
#pragma unroll
  for (int kk = 0; kk < 4; ++kk) {
    f32x4 o[2][2];
#pragma unroll
    for (int e2 = 0; e2 < 2; ++e2)
#pragma unroll
      for (int qt = 0; qt < 2; ++qt) {
        o[qt][e2] = mfma16(vmf[2 * kk + e2], p0[qt], (f32x4){0, 0, 0, 0});
        o[qt][e2] = mfma32(vx[2 * kk + e2], p12[qt], o[qt][e2]);
      }
#pragma unroll
    for (int qt = 0; qt < 2; ++qt)
      st16(rs, (unsigned)OFF_OX + ((((seg * 2 + qt) * NH + head) * 4 + kk) * 64 + lane) * 16, cat(pk4(o[qt][0]), pk4(o[qt][1])));
  }
  signal_cnt(ws, C_X + seg);
}

__device__ __forceinline__ void fastkv_task(const bf16* __restrict__ xn, const bf16* __restrict__ Wk, const bf16* __restrict__ Wv,
                                            char* lds, rsrc_t rs, int t, int tid) {
  const int lane = tid & 63, w = tid >> 6, li = lane & 15, g = lane >> 4, q4 = li >> 2, p4 = li & 3;
  const int isv = t >> 6, head = (t >> 1) & 31, hh = t & 1, tt = w >> 2, nt = w & 3;
  const bf16* W = (isv ? Wv : Wk) + head * DH + hh * 64;
  constexpr int P = 144;
#pragma unroll
  for (int u = 0; u < 8; ++u) {
    const int rr = 64 * u + (tid >> 3), ch = tid & 7;
    *(u32x4*)(lds + rr * P + ch * 16) = *(const u32x4*)(W + (size_t)rr * (NH * DH) + ch * 8);
  }
  bf16x8 af[16];
#pragma unroll
  for (int ks = 0; ks < 16; ++ks) af[ks] = *(const bf16x8*)(xn + (size_t)(16 * tt + li) * DM + 32 * ks + 8 * g);
  __syncthreads();
  f32x4 acc = {0, 0, 0, 0};
#pragma unroll
  for (int ks = 0; ks < 16; ++ks) {
    const bf16x8 bfr_ = cat(tr_read(lds, (32 * ks + 8 * g + q4) * P + (16 * nt + 4 * p4) * 2), tr_read(lds, (32 * ks + 8 * g + 4 + q4) * P + (16 * nt + 4 * p4) * 2));
    if (isv) acc = mfma32(af[ks], bfr_, acc); else acc = mfma32(bfr_, af[ks], acc);
  }
  if (isv) st8(rs, (unsigned)OFF_VF + ((head * 8 + hh * 4 + nt) * 64 + lane) * 16 + tt * 8, pk4(acc));
  else st8(rs, (unsigned)OFF_KF + (((head * 2 + tt) * 4 + hh * 2 + (nt >> 1)) * 64 + lane) * 16 + (nt & 1) * 8, pk4(acc));
}

__device__ __forceinline__ void wof_task(const bf16* __restrict__ Wo, char* lds, rsrc_t rs, int t, int tid) {
  const int lane = tid & 63, w = tid >> 6, li = lane & 15, g = lane >> 4, q4 = li >> 2, p4 = li & 3;
  const int h = t >> 1, chh = t & 1;
  constexpr int P = 528;
#pragma unroll
  for (int u = 0; u < 8; ++u) {
    const int cidx = tid + 512 * u, rr = cidx >> 5, ch = cidx & 31;
    *(u32x4*)(lds + rr * P + ch * 16) = *(const u32x4*)(Wo + ((size_t)h * DH + rr) * DM + chh * 256 + ch * 8);
  }
  __syncthreads();
#pragma unroll
  for (int n2 = 0; n2 < 2; ++n2)
#pragma unroll
    for (int kk = 0; kk < 4; ++kk) {
      const int ntl = 2 * w + n2;
      bf16x4 lo = tr_read(lds, ((2 * kk + 0) * 16 + 4 * g + q4) * P + (16 * ntl + 4 * p4) * 2);
      bf16x4 hi = tr_read(lds, ((2 * kk + 1) * 16 + 4 * g + q4) * P + (16 * ntl + 4 * p4) * 2);
      st16(rs, (unsigned)OFF_WOF + ((((chh * 16 + ntl) * NH + h) * 4 + kk) * 64 + lane) * 16, cat(lo, hi));
    }
}

__device__ __forceinline__ void final_task(char* ws, rsrc_t rs, char* lds, const bf16* __restrict__ x, bf16* __restrict__ out, int seg, int cb, int tid) {
  const int lane = tid & 63, w = __builtin_amdgcn_readfirstlane(tid >> 6);
  if (tid == 0) { wait_cnt(ws, C_WOF, 64); wait_cnt(ws, C_BO + (seg >> 2), 4); }
  __syncthreads();
  bf16x8 bf[16][2];
#pragma unroll
  for (int i = 0; i < 16; ++i)
#pragma unroll
    for (int nt = 0; nt < 2; ++nt) bf[i][nt] = ld16(rs, lane * 16, (unsigned)OFF_WOF + ((cb * 2 + nt) * 128 + w * 16 + i) * 1024);
  const int tt_ = (tid >> 7) & 1, nt_ = (tid >> 6) & 1, li = lane & 15, g = lane >> 4;
  const int row = seg * 32 + tt_ * 16 + li, col = cb * 32 + nt_ * 16 + 4 * g;
  const bf16x4 xv = *(const bf16x4*)(x + (size_t)row * DM + col);
  const bf16x4 bv = ld8(rs, (row * DM + col) * 2, (unsigned)OFF_BOUT);
  if (tid == 0) wait_cnt(ws, C_X + seg, 4);
  __syncthreads();
  f32x4 acc[2][2];
#pragma unroll
  for (int a = 0; a < 2; ++a) { acc[a][0] = (f32x4){0.f, 0.f, 0.f, 0.f}; acc[a][1] = (f32x4){0.f, 0.f, 0.f, 0.f}; }
#pragma unroll
  for (int i4 = 0; i4 < 4; ++i4) {
    bf16x8 af[4][2];
#pragma unroll
    for (int i = 0; i < 4; ++i)
#pragma unroll
      for (int tt = 0; tt < 2; ++tt) af[i][tt] = ld16(rs, lane * 16, (unsigned)OFF_OX + ((seg * 2 + tt) * 128 + w * 16 + i4 * 4 + i) * 1024);
#pragma unroll
    for (int i = 0; i < 4; ++i)
#pragma unroll
      for (int tt = 0; tt < 2; ++tt)
#pragma unroll
        for (int nt = 0; nt < 2; ++nt) acc[tt][nt] = mfma32(bf[i4 * 4 + i][nt], af[i][tt], acc[tt][nt]);
    if (i4 == 1) __builtin_amdgcn_sched_barrier(0);
  }
#pragma unroll
  for (int tt = 0; tt < 2; ++tt)
#pragma unroll
    for (int nt = 0; nt < 2; ++nt) *(f32x4*)(lds + (((w * 2 + tt) * 2 + nt) * 64 + lane) * 16) = acc[tt][nt];
  __syncthreads();
  if (tid < 256) {
    f32x4 a = {0, 0, 0, 0};
#pragma unroll
    for (int ww = 0; ww < 8; ++ww) a += *(const f32x4*)(lds + (((ww * 2 + tt_) * 2 + nt_) * 64 + lane) * 16);
    f32x4 o;
#pragma unroll
    for (int r = 0; r < 4; ++r) o[r] = bfr((float)xv[r] + bfr(a[r])) + (float)bv[r];
    *(bf16x4*)(out + (size_t)row * DM + col) = pk4(o);
  }
}

constexpr int T_F = 0, T_KV = 128, T_Q = T_KV + 1024, T_G = T_Q + 512, T_W = T_G + 448, T_B = T_W + 64, T_S = T_B + 64, T_END = T_S + 64 * 20;

__device__ __forceinline__ void worker(const bf16* __restrict__ x, const bf16* __restrict__ Wq, const bf16* __restrict__ Wk, const bf16* __restrict__ Wv,
                                       const bf16* __restrict__ Wo, const bf16* __restrict__ Wg, const bf16* __restrict__ Wu, const bf16* __restrict__ Wd,
                                       bf16* __restrict__ out, char* ws, char* lds) {
  const rsrc_t rs = __builtin_amdgcn_make_buffer_rsrc(ws, 0, WS_SPAN, 0x00020000);
  const bf16* xn = (const bf16*)(ws + OFF_XN);
  volatile int* tk = (volatile int*)(lds + 144 * 1024);
  for (;;) {
    __syncthreads();
    if (threadIdx.x == 0) *tk = (int)__hip_atomic_fetch_add(cnt_ptr(ws, C_TICKET), 1u, RLX_AGENT);
    __syncthreads();
    const int t = *tk;
    int tid = threadIdx.x; asm volatile("" : "+v"(tid));
    if (t >= T_END) break;
    if (t < T_KV) {
      fastkv_task(xn, Wk, Wv, lds, rs, t, tid);
      signal_cnt(ws, C_F);
    } else if (t < T_Q) {
      const int rb = (t - T_KV) >> 6, cb = t & 63;
      if (cb < 32) gemm_tile<0>(xn, DM, Wk, nullptr, NH * DH, DM, rb * 128, cb * 128, lds, rs, (unsigned)OFF_KF, tid);
      else gemm_tile<1>(xn, DM, Wv, nullptr, NH * DH, DM, rb * 128, (cb - 32) * 128, lds, rs, (unsigned)OFF_VF, tid);
      signal_cnt(ws, C_KV + rb);
    } else if (t < T_G) {
      const int u = t - T_Q, rb = u >> 5, cb = u & 31;
      gemm_tile<0>(xn, DM, Wq, nullptr, NH * DH, DM, rb * 128, cb * 128, lds, rs, (unsigned)OFF_QF, tid);
      signal_cnt(ws, C_Q + rb);
    } else if (t < T_W) {
      const int u = t - T_G, rb = u / 28, cb = u - rb * 28;
      gemm_tile<2>(xn, DM, Wg, Wu, FF, DM, rb * 128, cb * 64, lds, rs, (unsigned)OFF_H, tid);
      signal_cnt(ws, C_H + rb);
    } else if (t < T_B) {
      wof_task(Wo, lds, rs, t - T_W, tid);
      signal_cnt(ws, C_WOF);
    } else if (t < T_S) {
      const int u = t - T_B, rb = u >> 2, cb = u & 3;
      if (threadIdx.x == 0) wait_cnt(ws, C_H + rb, 28);
      acquire_all();
      gemm_tile<3>((const bf16*)(ws + OFF_H), FF, Wd, nullptr, DM, FF, rb * 128, cb * 128, lds, rs, (unsigned)OFF_BOUT, tid);
      signal_cnt(ws, C_BO + rb);
    } else {
      const int u = t - T_S, seg = u / 20, k = u - seg * 20;
      if (k < 4) xattn_task(ws, rs, seg, k * 8 + __builtin_amdgcn_readfirstlane(tid >> 6), tid);
      else final_task(ws, rs, lds, x, out, seg, k - 4, tid);
    }
  }
}

constexpr int CL_SCR = 0;
constexpr int CL_KX = 0;
constexpr int CL_VX = CL_KX + 8192;
constexpr int CL_MEM = CL_VX + 4096;
constexpr int CL_QF = CL_MEM + 8192;
constexpr int CL_KF = CL_QF + 4096;
constexpr int CL_OF = CL_KF + 4096;
constexpr int CL_P = CL_OF + 2048;
constexpr int CL_WV = 32768;
constexpr int CL_WO = 98304;
constexpr int CHAIN_LDS = 163840;
#define STAMP(k)
constexpr unsigned SENT = 0xFFFFFFFFu;
constexpr unsigned SPIN_CAP = 1u << 18;

__device__ __forceinline__ void chain(const bf16* __restrict__ Wq, const bf16* __restrict__ Wk, const bf16* __restrict__ Wv,
                                      const bf16* __restrict__ Wo, char* ws, char* lds) {
  const int tid = threadIdx.x, lane = tid & 63, w = __builtin_amdgcn_readfirstlane(tid >> 6);
  const int li = lane & 15, g = lane >> 4, q4 = li >> 2, p4 = li & 3;
  const int b = blockIdx.x, h = b >> 1, c = b & 1;
  bf16x8 wqk[2][16];
  {
    constexpr int P1 = 272, P2 = 1040, P3 = 144;
    u32x4 r[8];
    auto ld_qk = [&](int it) {
#pragma unroll
      for (int u = 0; u < 8; ++u) {
        const int cidx = tid + 512 * u, mat = cidx >> 11, rr = (cidx >> 4) & 127, ch = cidx & 15;
        r[u] = *(const u32x4*)((mat ? Wk : Wq) + (size_t)(128 * it + rr) * (NH * DH) + h * DH + ch * 8);
      }
    };
    auto ld_wo = [&]() {
#pragma unroll
      for (int u = 0; u < 8; ++u) {
        const int cidx = tid + 512 * u, rr = cidx >> 6, ch = cidx & 63;
        r[u] = *(const u32x4*)(Wo + ((size_t)h * DH + c * 64 + rr) * DM + ch * 8);
      }
    };
    auto ld_wv = [&]() {
#pragma unroll
      for (int u = 0; u < 8; ++u) {
        const int rr = 64 * u + (tid >> 3), ch = tid & 7;
        r[u] = *(const u32x4*)(Wv + (size_t)rr * (NH * DH) + h * DH + c * 64 + ch * 8);
      }
    };
    char* scr = lds + 32768;
    ld_qk(0);
#pragma unroll
    for (int it = 0; it < 4; ++it) {
#pragma unroll
      for (int u = 0; u < 8; ++u) {
        const int cidx = tid + 512 * u, mat = cidx >> 11, rr = (cidx >> 4) & 127, ch = cidx & 15;
        *(u32x4*)(scr + mat * (128 * P1) + rr * P1 + ch * 16) = r[u];
      }
      __syncthreads();
      if (it < 3) ld_qk(it + 1); else ld_wo();
      const char* base = scr + (w >> 2) * (128 * P1);
#pragma unroll
      for (int k4 = 0; k4 < 4; ++k4)
#pragma unroll
        for (int t = 0; t < 2; ++t) {
          const int c16 = 2 * (w & 3) + t;
          bf16x4 lo = tr_read(base, (32 * k4 + 8 * g + q4) * P1 + (16 * c16 + 4 * p4) * 2);
          bf16x4 hi = tr_read(base, (32 * k4 + 8 * g + 4 + q4) * P1 + (16 * c16 + 4 * p4) * 2);
          wqk[t][4 * it + k4] = cat(lo, hi);
        }
      __syncthreads();
    }
    scr = lds;
#pragma unroll
    for (int u = 0; u < 8; ++u) {
      const int cidx = tid + 512 * u, rr = cidx >> 6, ch = cidx & 63;
      *(u32x4*)(scr + rr * P2 + ch * 16) = r[u];
    }
    __syncthreads();
    ld_wv();
#pragma unroll
    for (int nt = 0; nt < 4; ++nt)
#pragma unroll
      for (int k2 = 0; k2 < 2; ++k2) {
        const int col = 64 * w + 16 * nt + 4 * p4;
        bf16x4 lo = tr_read(scr, ((2 * k2 + 0) * 16 + 4 * g + q4) * P2 + col * 2);
        bf16x4 hi = tr_read(scr, ((2 * k2 + 1) * 16 + 4 * g + q4) * P2 + col * 2);
        *(bf16x8*)(lds + CL_WO + (((w * 4 + nt) * 2 + k2) * 64 + lane) * 16) = cat(lo, hi);
      }
    __syncthreads();
#pragma unroll
    for (int it = 0; it < 4; ++it) {
#pragma unroll
      for (int u = 0; u < 2; ++u) *(u32x4*)(scr + (64 * u + (tid >> 3)) * P3 + (tid & 7) * 16) = r[2 * it + u];
      __syncthreads();
#pragma unroll
      for (int e2 = 0; e2 < 2; ++e2) {
        const int et = 2 * (w & 1) + e2, k4 = w >> 1;
        bf16x4 lo = tr_read(scr, (32 * k4 + 8 * g + q4) * P3 + (16 * et + 4 * p4) * 2);
        bf16x4 hi = tr_read(scr, (32 * k4 + 8 * g + 4 + q4) * P3 + (16 * et + 4 * p4) * 2);
        *(bf16x8*)(lds + CL_WV + ((et * 16 + 4 * it + k4) * 64 + lane) * 16) = cat(lo, hi);
      }
      __syncthreads();
    }
  }
  const auto rs_mem = __builtin_amdgcn_make_buffer_rsrc(ws + OFF_MEMH, 0, 65 * 8192, 0x00020000);
  const auto rs_part = __builtin_amdgcn_make_buffer_rsrc(ws + OFF_PART, 0, 1 << 20, 0x00020000);
  const auto rs_km = __builtin_amdgcn_make_buffer_rsrc(ws + OFF_KMH, 0, 16 << 20, 0x00020000);
  const float scale = 0.08838834764831845f;
  const auto rs_kv = __builtin_amdgcn_make_buffer_rsrc(ws + OFF_KF, 0, 32 << 20, 0x00020000);
  unsigned rdy = 0;
  if (w < 4) {
    unsigned spins = 0;
    while (__builtin_amdgcn_readfirstlane(__hip_atomic_load(cnt_ptr(ws, C_F), RLX_AGENT)) < 128u) { __builtin_amdgcn_s_sleep(4); if (++spins > SPIN_CAP) break; }
    const u32x4 a0 = __builtin_amdgcn_raw_buffer_load_b128(rs_kv, lane * 16, h * 8192 + w * 2048, 16);
    const u32x4 a1 = __builtin_amdgcn_raw_buffer_load_b128(rs_kv, lane * 16, h * 8192 + w * 2048 + 1024, 16);
    const u32x4 a2 = __builtin_amdgcn_raw_buffer_load_b128(rs_kv, lane * 16, (16 << 20) + h * 8192 + c * 4096 + w * 1024, 16);
    *(u32x4*)(lds + lane * 16 + CL_KX + w * 2048) = a0;
    *(u32x4*)(lds + lane * 16 + CL_KX + w * 2048 + 1024) = a1;
    *(u32x4*)(lds + lane * 16 + CL_VX + w * 1024) = a2;
    rdy = 0u;
  }
  const int pst_off = (((8 * w + 2 * (g & 1) + (g >> 1)) * 64 + b) * 8 + li) * 16;
  const char* l16 = lds + lane * 16;
  const char* mfb = lds + CL_MEM + (g * 8 + (li & 7)) * 16;
  const char* wvb = lds + CL_WV + ((w & 3) * 1024 + lane) * 16;
  const char* wob = lds + CL_WO + (w * 512 + lane) * 16;
  for (int s = 0; s < NSEG; ++s) {
    if (w >= 6) {
      u32x4 v[4]; unsigned spins = 0;
      asm volatile("s_waitcnt vmcnt(0)" ::: "memory");
      for (;;) {
        asm volatile("" ::: "memory");
#pragma unroll
        for (int u = 0; u < 4; ++u) v[u] = __builtin_amdgcn_raw_buffer_load_b128(rs_mem, lane * 16, s * 8192 + (w - 6) * 4096 + u * 1024, 16);
        unsigned m = 0;
#pragma unroll
        for (int u = 0; u < 4; ++u) m = max(m, max(v[u][0], v[u][2]));
        if (__all(m != SENT)) break;
        if (++spins > SPIN_CAP) break;
      }
#pragma unroll
      for (int u = 0; u < 4; ++u) *(u32x4*)(l16 + CL_MEM + (w - 6) * 4096 + u * 1024) = v[u];
    }
    STAMP(0);
    __syncthreads();
    STAMP(1);
    if (tid == 256 && s > 0) __hip_atomic_store(cnt_ptr(ws, C_CH + s - 1) + b, 1u, RLX_AGENT);
    u32x4 pf[3];
    const int sn = (s + 1 < NSEG) ? s + 1 : s;
    const int so = (sn * NH + h) * 8192;
    bool early = true;
    if (w < 4) {
      early = __builtin_amdgcn_readfirstlane(rdy) >= 64u;
      pf[0] = __builtin_amdgcn_raw_buffer_load_b128(rs_kv, lane * 16, so + w * 2048, 16);
      pf[1] = __builtin_amdgcn_raw_buffer_load_b128(rs_kv, lane * 16, so + w * 2048 + 1024, 16);
      pf[2] = __builtin_amdgcn_raw_buffer_load_b128(rs_kv, lane * 16, (16 << 20) + so + c * 4096 + w * 1024, 16);
      const int s2 = (s + 2 < NSEG) ? s + 2 : NSEG - 1;
      rdy = __hip_atomic_load(cnt_ptr(ws, C_KV + (s2 >> 2)), RLX_AGENT);
    }
    bf16x8 mf[16];
#pragma unroll
    for (int ks = 0; ks < 16; ++ks) mf[ks] = *(const bf16x8*)(mfb + ks * 512);
    __builtin_amdgcn_sched_barrier(0);
    {
      f32x4 a0 = {0, 0, 0, 0}, a1 = {0, 0, 0, 0};
#pragma unroll
      for (int ks = 0; ks < 16; ++ks) {
        a0 = mfma32(wqk[0][ks], mf[ks], a0);
        a1 = mfma32(wqk[1][ks], mf[ks], a1);
      }
      const bf16x8 o = cat(pk4(a0), pk4(a1));
      *(bf16x8*)(lds + (w < 4 ? CL_QF : CL_KF) + ((w & 3) * 64 + lane) * 16) = o;
      if (w >= 4 && c == 0) __builtin_amdgcn_raw_buffer_store_b128(__builtin_bit_cast(u32x4, o), rs_km, lane * 16, ((s * NH + h) * 4 + (w & 3)) * 1024, 16);
    }
    STAMP(2);
    bf16x4 vm4 = {(__bf16)0.f, (__bf16)0.f, (__bf16)0.f, (__bf16)0.f};
    if (w == 0) {
      bf16x8 kxf[8];
#pragma unroll
      for (int u = 0; u < 8; ++u) kxf[u] = *(const bf16x8*)(l16 + CL_KX + u * 1024);
      __syncthreads();
      f32x4 s0 = {0, 0, 0, 0}, s1 = {0, 0, 0, 0}, s2 = {0, 0, 0, 0};
#pragma unroll
      for (int kk = 0; kk < 4; ++kk) {
        const bf16x8 qf = *(const bf16x8*)(l16 + CL_QF + kk * 1024);
        const bf16x8 k0 = *(const bf16x8*)(l16 + CL_KF + kk * 1024);
        s0 = mfma32(k0, qf, s0); s1 = mfma32(kxf[kk], qf, s1); s2 = mfma32(kxf[4 + kk], qf, s2);
      }
      if (g >= 2) s0 = (f32x4){-3.0e38f, -3.0e38f, -3.0e38f, -3.0e38f};
      float m = fmaxf(fmaxf(fmaxf(s0[0], s0[1]), fmaxf(s0[2], s0[3])), fmaxf(fmaxf(fmaxf(s1[0], s1[1]), fmaxf(s1[2], s1[3])), fmaxf(fmaxf(s2[0], s2[1]), fmaxf(s2[2], s2[3]))));
      m = xmax16_32(m);
      const float cexp = scale * 1.4426950408889634f, mc = -m * cexp;
      bf16x4 p0; bf16x8 p12;
#pragma unroll
      for (int r = 0; r < 4; ++r) {
        p0[r] = (__bf16)__builtin_amdgcn_exp2f(fmaf(s0[r], cexp, mc));
        p12[r] = (__bf16)__builtin_amdgcn_exp2f(fmaf(s1[r], cexp, mc));
        p12[4 + r] = (__bf16)__builtin_amdgcn_exp2f(fmaf(s2[r], cexp, mc));
      }
      *(bf16x8*)(lds + CL_P + lane * 16) = p12;
      *(bf16x4*)(lds + CL_P + 1024 + lane * 8) = p0;
    } else {
      if (w >= 4) {
        f32x4 va = {0, 0, 0, 0}, vb = {0, 0, 0, 0};
        bf16x8 wv[2][4];
#pragma unroll
        for (int k1 = 0; k1 < 4; ++k1) wv[0][k1] = *(const bf16x8*)(wvb + k1 * 1024);
        __syncthreads();
#pragma unroll
        for (int k4 = 0; k4 < 4; ++k4) {
          if (k4 < 3) {
#pragma unroll
            for (int k1 = 0; k1 < 4; ++k1) wv[(k4 + 1) & 1][k1] = *(const bf16x8*)(wvb + (4 * k4 + 4 + k1) * 1024);
          }
#pragma unroll
          for (int k1 = 0; k1 < 4; k1 += 2) { va = mfma32(mf[4 * k4 + k1], wv[k4 & 1][k1], va); vb = mfma32(mf[4 * k4 + k1 + 1], wv[k4 & 1][k1 + 1], vb); }
          __builtin_amdgcn_sched_barrier(0);
        }
        vm4 = pk4(va + vb);
        __builtin_amdgcn_raw_buffer_store_b64(__builtin_bit_cast(u32x2, vm4), rs_km, lane * 8, (8 << 20) + ((s * NH + h) * 8 + 4 * c + (w - 4)) * 512, 16);
      } else {
        __syncthreads();
      }
    }
    bf16x8 wo[4][2];
#pragma unroll
    for (int nt = 0; nt < 4; ++nt)
#pragma unroll
      for (int k2 = 0; k2 < 2; ++k2) wo[nt][k2] = *(const bf16x8*)(wob + (nt * 2 + k2) * 1024);
    __syncthreads();
    if (w >= 4) {
      const bf16x8 p12 = *(const bf16x8*)(lds + CL_P + lane * 16);
      const bf16x4 p0 = *(const bf16x4*)(lds + CL_P + 1024 + lane * 8);
      const bf16x8 vx8 = *(const bf16x8*)(lds + CL_VX + ((w - 4) * 64 + lane) * 16);
      f32x4 o = mfma16(vm4, p0, (f32x4){0, 0, 0, 0});
      const bf16x4 one4 = {(__bf16)1.f, (__bf16)1.f, (__bf16)1.f, (__bf16)1.f};
      f32x4 l = mfma16(one4, p0, (f32x4){0, 0, 0, 0});
      o = mfma32(vx8, p12, o);
      l = mfma32(cat(one4, one4), p12, l);
      const float inv = __builtin_amdgcn_rcpf(l[0]);
      *(bf16x4*)(lds + CL_OF + (((w - 4) >> 1) * 64 + lane) * 16 + (w & 1) * 8) = pk4(o * inv);
    }
    STAMP(3);
    __syncthreads();
    STAMP(4);
    if (s + 1 == NSEG) {
      if (w >= 4) asm volatile("s_waitcnt vmcnt(0)" ::: "memory");
      __syncthreads();
      if (tid == 64) __hip_atomic_store(cnt_ptr(ws, C_CH + s) + b, 1u, RLX_AGENT);
      break;
    }
    const int slot = (s & 1) << 19;
    {
      const bf16x8 of0 = *(const bf16x8*)(l16 + CL_OF);
      const bf16x8 of1 = *(const bf16x8*)(l16 + CL_OF + 1024);
#pragma unroll
      for (int n2 = 0; n2 < 2; ++n2) {
        f32x4 pa = mfma32(wo[2 * n2][0], of0, (f32x4){0, 0, 0, 0});
        f32x4 pb = mfma32(wo[2 * n2 + 1][0], of0, (f32x4){0, 0, 0, 0});
        pa = mfma32(wo[2 * n2][1], of1, pa);
        pb = mfma32(wo[2 * n2 + 1][1], of1, pb);
        u32x2 A = __builtin_bit_cast(u32x2, pk4(pa)), B = __builtin_bit_cast(u32x2, pk4(pb));
        auto r0 = __builtin_amdgcn_permlane16_swap(A[0], B[0], false, false);
        auto r1 = __builtin_amdgcn_permlane16_swap(A[1], B[1], false, false);
        u32x4 pv = {r0[0] | 1u, r1[0], r0[1] | 1u, r1[1]};
        if (li < 8) __builtin_amdgcn_raw_buffer_store_b128(pv, rs_part, pst_off, slot + n2 * 32768, 16);
      }
    }
    STAMP(5);
    if (w < 4) {
      if (!early) {
        unsigned spins = 0;
        while (__builtin_amdgcn_readfirstlane(__hip_atomic_load(cnt_ptr(ws, C_KV + (sn >> 2)), RLX_AGENT)) < 64u) { __builtin_amdgcn_s_sleep(2); if (++spins > SPIN_CAP) break; }
        const u32x4 t0 = __builtin_amdgcn_raw_buffer_load_b128(rs_kv, lane * 16, so + w * 2048, 16);
        const u32x4 t1 = __builtin_amdgcn_raw_buffer_load_b128(rs_kv, lane * 16, so + w * 2048 + 1024, 16);
        const u32x4 t2 = __builtin_amdgcn_raw_buffer_load_b128(rs_kv, lane * 16, (16 << 20) + so + c * 4096 + w * 1024, 16);
        *(u32x4*)(l16 + CL_KX + w * 2048) = t0;
        *(u32x4*)(l16 + CL_KX + w * 2048 + 1024) = t1;
        *(u32x4*)(l16 + CL_VX + w * 1024) = t2;
      } else {
        *(u32x4*)(l16 + CL_KX + w * 2048) = pf[0];
        *(u32x4*)(l16 + CL_KX + w * 2048 + 1024) = pf[1];
        *(u32x4*)(l16 + CL_VX + w * 1024) = pf[2];
      }
    }
    if (w == 4 || w == 5) asm volatile("s_waitcnt vmcnt(0)" ::: "memory");
    if (w >= 6) {
      u32x2 v[8]; unsigned spins = 0;
      const int roff = lane * 16 + (w - 6) * 8;
      asm volatile("s_waitcnt vmcnt(0)" ::: "memory");
      for (;;) {
        asm volatile("" ::: "memory");
#pragma unroll
        for (int it = 0; it < 8; ++it) v[it] = __builtin_amdgcn_raw_buffer_load_b64(rs_part, roff, slot + b * 8192 + it * 1024, 16);
        unsigned m = 1u;
#pragma unroll
        for (int it = 0; it < 8; ++it) m &= v[it][0];
        if (__all(m != 0u)) break;
        if (++spins > SPIN_CAP) break;
      }
      STAMP(6);
      f32x4 a = {0, 0, 0, 0};
#pragma unroll
      for (int it = 0; it < 8; ++it) {
        a[0] += __builtin_bit_cast(float, v[it][0] << 16); a[1] += __builtin_bit_cast(float, v[it][0] & 0xffff0000u);
        a[2] += __builtin_bit_cast(float, v[it][1] << 16); a[3] += __builtin_bit_cast(float, v[it][1] & 0xffff0000u);
      }
#pragma unroll
      for (int r = 0; r < 4; ++r) a[r] = xsum8_16_32(a[r]);
      u32x2 o = __builtin_bit_cast(u32x2, pk4(a));
      if (o[0] == SENT) o[0] = 0x7FFF7FFFu;
      if (lane < 8) __builtin_amdgcn_raw_buffer_store_b64(o, rs_mem, lane * 16 + (w - 6) * 8, (s + 1) * 8192 + b * 128, 16);
      const u32x2 z = {0u, 0u};
#pragma unroll
      for (int it = 0; it < 8; ++it) __builtin_amdgcn_raw_buffer_store_b64(z, rs_part, roff, slot + b * 8192 + it * 1024, 16);
      STAMP(7);
    }
  }
}

__global__ __launch_bounds__(512, 2) void k_main(const bf16* __restrict__ x, const bf16* __restrict__ Wq, const bf16* __restrict__ Wk,
                                                 const bf16* __restrict__ Wv, const bf16* __restrict__ Wo, const bf16* __restrict__ Wg,
                                                 const bf16* __restrict__ Wu, const bf16* __restrict__ Wd, bf16* __restrict__ out, char* ws) {
  extern __shared__ __attribute__((aligned(16))) char lds[];
  if (blockIdx.x < 64) chain(Wq, Wk, Wv, Wo, ws, lds);
  else worker(x, Wq, Wk, Wv, Wo, Wg, Wu, Wd, out, ws, lds);
}

extern "C" void kernel_launch(void* const* d_in, const int* in_sizes, int n_in,
                              void* d_out, int out_size, void* d_ws, size_t ws_size,
                              hipStream_t stream) {
  const bf16* x = (const bf16*)d_in[0];
  const bf16* mem0 = (const bf16*)d_in[1];
  const bf16* Wq = (const bf16*)d_in[2];
  const bf16* Wk = (const bf16*)d_in[3];
  const bf16* Wv = (const bf16*)d_in[4];
  const bf16* Wo = (const bf16*)d_in[5];
  const bf16* Wg = (const bf16*)d_in[8];
  const bf16* Wu = (const bf16*)d_in[9];
  const bf16* Wd = (const bf16*)d_in[10];
  char* ws = (char*)d_ws;
  (void)hipFuncSetAttribute((const void*)k_main, hipFuncAttributeMaxDynamicSharedMemorySize, CHAIN_LDS);
  k_ln<<<S_ / 4, 256, 0, stream>>>(x, mem0, ws);
  k_main<<<256, 512, CHAIN_LDS, stream>>>(x, Wq, Wk, Wv, Wo, Wg, Wu, Wd, (bf16*)d_out, ws);
}
```

```cpp
#include <hip/hip_runtime.h>
#include <hip/hip_bf16.h>

constexpr int S_ = 2048, DM = 512, NH = 32, DH = 128, MM = 8, SEG = 32, NSEG = 64, FF = 1792;

typedef __bf16 bf16;
typedef __bf16 bf16x8 __attribute__((ext_vector_type(8)));
typedef __bf16 bf16x4 __attribute__((ext_vector_type(4)));
typedef short s16x4 __attribute__((ext_vector_type(4)));
typedef float f32x4 __attribute__((ext_vector_type(4)));
typedef unsigned u32x4 __attribute__((ext_vector_type(4)));
typedef unsigned u32x2 __attribute__((ext_vector_type(2)));
#define LAS __attribute__((address_space(3)))

constexpr size_t OFF_MEMH = 0;
constexpr size_t OFF_PART = 1 << 20;
constexpr size_t OFF_CNT  = 2 << 20;
constexpr size_t OFF_XN   = 4u << 20;
constexpr size_t OFF_BOUT = 6u << 20;
constexpr size_t OFF_H    = 8u << 20;
constexpr size_t OFF_QF   = 16u << 20;
constexpr size_t OFF_KF   = 32u << 20;
constexpr size_t OFF_VF   = 48u << 20;
constexpr size_t OFF_OX   = 64u << 20;
constexpr size_t OFF_KMH  = 80u << 20;
constexpr size_t OFF_VMH  = 88u << 20;
constexpr size_t OFF_WOF  = 96u << 20;
constexpr unsigned WS_SPAN = 100u << 20;
constexpr int C_TICKET = 0, C_KV = 1, C_Q = 17, C_H = 33, C_BO = 49, C_WOF = 65, C_X = 66, C_CH = 130, C_F = 194;
#define RLX_AGENT __ATOMIC_RELAXED, __HIP_MEMORY_SCOPE_AGENT

__device__ __forceinline__ f32x4 mfma32(bf16x8 a, bf16x8 b, f32x4 c) {
  return __builtin_amdgcn_mfma_f32_16x16x32_bf16(a, b, c, 0, 0, 0);
}
__device__ __forceinline__ f32x4 mfma16(bf16x4 a, bf16x4 b, f32x4 c) {
  bf16x8 a8, b8;
#pragma unroll
  for (int j = 0; j < 4; ++j) { a8[j] = a[j]; b8[j] = b[j]; a8[4 + j] = (__bf16)0.f; b8[4 + j] = (__bf16)0.f; }
  return __builtin_amdgcn_mfma_f32_16x16x32_bf16(a8, b8, c, 0, 0, 0);
}
__device__ __forceinline__ float bfr(float x) { return (float)(__bf16)x; }
__device__ __forceinline__ bf16x4 tr_read(const char* lds_base, unsigned off) {
  s16x4 t = __builtin_amdgcn_ds_read_tr16_b64_v4i16((LAS s16x4*)(lds_base + off));
  return __builtin_bit_cast(bf16x4, t);
}
__device__ __forceinline__ bf16x8 cat(bf16x4 a, bf16x4 b) {
  bf16x8 r; r[0] = a[0]; r[1] = a[1]; r[2] = a[2]; r[3] = a[3]; r[4] = b[0]; r[5] = b[1]; r[6] = b[2]; r[7] = b[3]; return r;
}
__device__ __forceinline__ bf16x4 pk4(f32x4 v) { bf16x4 r; r[0] = (__bf16)v[0]; r[1] = (__bf16)v[1]; r[2] = (__bf16)v[2]; r[3] = (__bf16)v[3]; return r; }

__device__ __forceinline__ float xmax16_32(float x) {
  unsigned u = __builtin_bit_cast(unsigned, x);
  auto a = __builtin_amdgcn_permlane16_swap(u, u, false, false);
  x = fmaxf(__builtin_bit_cast(float, (unsigned)a[0]), __builtin_bit_cast(float, (unsigned)a[1]));
  u = __builtin_bit_cast(unsigned, x);
  auto c = __builtin_amdgcn_permlane32_swap(u, u, false, false);
  return fmaxf(__builtin_bit_cast(float, (unsigned)c[0]), __builtin_bit_cast(float, (unsigned)c[1]));
}
__device__ __forceinline__ float xsum8_16_32(float x) {
  x += __builtin_bit_cast(float, __builtin_amdgcn_update_dpp(0, __builtin_bit_cast(int, x), 0x128, 0xf, 0xf, false));
  unsigned u = __builtin_bit_cast(unsigned, x);
  auto a = __builtin_amdgcn_permlane16_swap(u, u, false, false);
  x = __builtin_bit_cast(float, (unsigned)a[0]) + __builtin_bit_cast(float, (unsigned)a[1]);
  u = __builtin_bit_cast(unsigned, x);
  auto c = __builtin_amdgcn_permlane32_swap(u, u, false, false);
  return __builtin_bit_cast(float, (unsigned)c[0]) + __builtin_bit_cast(float, (unsigned)c[1]);
}

__global__ __launch_bounds__(256) void k_ln(const bf16* __restrict__ x, const bf16* __restrict__ mem0, char* ws) {
  const int lane = threadIdx.x & 63, row = blockIdx.x * 4 + (threadIdx.x >> 6);
  bf16* xn = (bf16*)(ws + OFF_XN);
  bf16x8 v = *(const bf16x8*)(x + (size_t)row * DM + lane * 8);
  float f[8], s = 0.f;
#pragma unroll
  for (int j = 0; j < 8; ++j) { f[j] = (float)v[j]; s += f[j]; }
#pragma unroll
  for (int m = 32; m >= 1; m >>= 1) s += __shfl_xor(s, m);
  const float mean = s * (1.f / DM);
  float q = 0.f;
#pragma unroll
  for (int j = 0; j < 8; ++j) { float d = f[j] - mean; q += d * d; }
#pragma unroll
  for (int m = 32; m >= 1; m >>= 1) q += __shfl_xor(q, m);
  const float mb = bfr(mean), vb = bfr(q * (1.f / DM));
  const float den = bfr(sqrtf(bfr(vb + 1e-5f)));
  bf16x8 o;
#pragma unroll
  for (int j = 0; j < 8; ++j) o[j] = (__bf16)(bfr(f[j] - mb) / den);
  *(bf16x8*)(xn + (size_t)row * DM + lane * 8) = o;
  {
    const u32x4 ff = {0xFFFFFFFFu, 0xFFFFFFFFu, 0xFFFFFFFFu, 0xFFFFFFFFu}, zz = {0u, 0u, 0u, 0u};
    if (threadIdx.x < 64) *(u32x4*)(ws + OFF_MEMH + 8192 + ((size_t)blockIdx.x * 64 + threadIdx.x) * 16) = ff;
    if (threadIdx.x < 136) *(u32x4*)(ws + OFF_PART + ((size_t)blockIdx.x * 136 + threadIdx.x) * 16) = zz;
  }
  if (blockIdx.x == 0) {
    for (int c = threadIdx.x; c < 512; c += 256) {
      const int i = c & 7, dg = c >> 3;
      *(bf16x8*)(ws + OFF_MEMH + (size_t)c * 16) = *(const bf16x8*)(mem0 + i * DM + dg * 8);
    }
  }
}

constexpr int GA_PITCH = 272;
constexpr int GA_BYTES = 128 * GA_PITCH;
constexpr int GB_BYTES = 128 * 256;
__device__ __forceinline__ unsigned off_b(unsigned row, unsigned ch) { return 256u * row + 16u * (ch ^ (((row & 3) << 2) | ((row >> 2) & 3))); }
typedef __amdgpu_buffer_rsrc_t rsrc_t;
__device__ __forceinline__ void st16(rsrc_t rs, unsigned off, bf16x8 v) { __builtin_amdgcn_raw_buffer_store_b128(__builtin_bit_cast(u32x4, v), rs, off, 0, 16); }
__device__ __forceinline__ void st8(rsrc_t rs, unsigned off, bf16x4 v) { __builtin_amdgcn_raw_buffer_store_b64(__builtin_bit_cast(u32x2, v), rs, off, 0, 16); }

__device__ __forceinline__ bf16x8 ld16(rsrc_t rs, unsigned voff, unsigned soff) { return __builtin_bit_cast(bf16x8, __builtin_amdgcn_raw_buffer_load_b128(rs, voff, soff, 16)); }
__device__ __forceinline__ bf16x4 ld8(rsrc_t rs, unsigned voff, unsigned soff) { return __builtin_bit_cast(bf16x4, __builtin_amdgcn_raw_buffer_load_b64(rs, voff, soff, 16)); }

template <int MODE>
__device__ __forceinline__ void gemm_tile(const bf16* __restrict__ A, int lda, const bf16* __restrict__ B0, const bf16* __restrict__ B1,
                                          int ldb, int K, int row0, int col0, char* lds, rsrc_t rs, unsigned dst, int tid) {
  const int lane = tid & 63, w = tid >> 6, wm = w >> 1, wn = w & 1;
  const int li = lane & 15, g = lane >> 4, q = li >> 2, p = li & 3;
  char* la = lds; char* lb = lds + GA_BYTES;
  f32x4 acc[2][4];
#pragma unroll
  for (int a = 0; a < 2; ++a)
#pragma unroll
    for (int b = 0; b < 4; ++b) acc[a][b] = (f32x4){0.f, 0.f, 0.f, 0.f};
  const bf16* asrc[4]; const bf16* bsrc[4]; unsigned adst[4], bdst[4];
#pragma unroll
  for (int u = 0; u < 4; ++u) {
    const int c = tid + 512 * u;
    asrc[u] = A + (size_t)(row0 + (c >> 4)) * lda + (c & 15) * 8;
    adst[u] = (c >> 4) * GA_PITCH + (c & 15) * 16;
    const int kr = c >> 4, ch = c & 15;
    if (MODE == 2) {
      const int blk = ch >> 2;
      bsrc[u] = ((blk & 1) ? B1 : B0) + (size_t)kr * ldb + col0 + (blk >> 1) * 32 + (ch & 3) * 8;
    } else {
      bsrc[u] = B0 + (size_t)kr * ldb + col0 + ch * 8;
    }
    bdst[u] = off_b(kr, ch);
  }
  auto compute = [&]() {
#pragma unroll
    for (int ks = 0; ks < 4; ++ks) {
      bf16x8 af[2], bfr_[4];
#pragma unroll
      for (int tt = 0; tt < 2; ++tt) af[tt] = *(const bf16x8*)(la + (wm * 32 + tt * 16 + li) * GA_PITCH + ks * 64 + g * 16);
#pragma unroll
      for (int nt = 0; nt < 4; ++nt) {
        const unsigned ch = 2 * (wn * 4 + nt) + (p >> 1);
        bf16x4 lo = tr_read(lb, off_b(32 * ks + 8 * g + q, ch) + 8 * (p & 1));
        bf16x4 hi = tr_read(lb, off_b(32 * ks + 8 * g + 4 + q, ch) + 8 * (p & 1));
        bfr_[nt] = cat(lo, hi);
      }
#pragma unroll
      for (int tt = 0; tt < 2; ++tt)
#pragma unroll
        for (int nt = 0; nt < 4; ++nt) {
          if (MODE == 1) acc[tt][nt] = mfma32(af[tt], bfr_[nt], acc[tt][nt]);
          else acc[tt][nt] = mfma32(bfr_[nt], af[tt], acc[tt][nt]);
        }
    }
  };
  {
    u32x4 ra[4], rb[4];
#pragma unroll
    for (int u = 0; u < 4; ++u) { ra[u] = *(const u32x4*)asrc[u]; rb[u] = *(const u32x4*)bsrc[u]; }
    const int nkt = K >> 7;
    for (int kt = 0; kt < nkt; ++kt) {
#pragma unroll
      for (int u = 0; u < 4; ++u) { *(u32x4*)(la + adst[u]) = ra[u]; *(u32x4*)(lb + bdst[u]) = rb[u]; }
      __syncthreads();
      if (kt + 1 < nkt) {
#pragma unroll
        for (int u = 0; u < 4; ++u) {
          ra[u] = *(const u32x4*)(asrc[u] + (kt + 1) * 128);
          rb[u] = *(const u32x4*)(bsrc[u] + (size_t)(kt + 1) * 128 * ldb);
        }
      }
      compute();
      __syncthreads();
    }
  }
  if (MODE == 0) {
    const int seg = (row0 >> 5) + wm, head = col0 >> 7;
#pragma unroll
    for (int tt = 0; tt < 2; ++tt)
#pragma unroll
      for (int k2 = 0; k2 < 2; ++k2)
        st16(rs, dst + ((((seg * NH + head) * 2 + tt) * 4 + (wn * 2 + k2)) * 64 + lane) * 16, cat(pk4(acc[tt][2 * k2]), pk4(acc[tt][2 * k2 + 1])));
  } else if (MODE == 1) {
    const int seg = (row0 >> 5) + wm, head = col0 >> 7;
#pragma unroll
    for (int nt = 0; nt < 4; ++nt)
      st16(rs, dst + (((seg * NH + head) * 8 + (wn * 4 + nt)) * 64 + lane) * 16, cat(pk4(acc[0][nt]), pk4(acc[1][nt])));
  } else if (MODE == 2) {
#pragma unroll
    for (int tt = 0; tt < 2; ++tt)
#pragma unroll
      for (int n2 = 0; n2 < 2; ++n2) {
        f32x4 hv;
#pragma unroll
        for (int r = 0; r < 4; ++r) {
          const float gg = bfr(acc[tt][n2][r]), uu = bfr(acc[tt][n2 + 2][r]);
          const float sg = bfr(1.f / (1.f + __expf(-gg)));
          hv[r] = sg * uu;
        }
        const int row = row0 + wm * 32 + tt * 16 + li, col = col0 + 32 * wn + 16 * n2 + 4 * g;
        st8(rs, dst + (row * FF + col) * 2, pk4(hv));
      }
  } else {
#pragma unroll
    for (int tt = 0; tt < 2; ++tt)
#pragma unroll
      for (int nt = 0; nt < 4; ++nt) {
        const int row = row0 + wm * 32 + tt * 16 + li, col = col0 + 64 * wn + 16 * nt + 4 * g;
        st8(rs, dst + (row * DM + col) * 2, pk4(acc[tt][nt]));
      }
  }
}

__device__ __forceinline__ unsigned* cnt_ptr(char* ws, int idx) { return (unsigned*)(ws + OFF_CNT) + idx * 64; }
__device__ __forceinline__ void wait_cnt(char* ws, int idx, unsigned target) {
  unsigned spins = 0;
  while (__hip_atomic_load(cnt_ptr(ws, idx), RLX_AGENT) < target) { __builtin_amdgcn_s_sleep(4); if (++spins > (1u << 18)) break; }
}
__device__ __forceinline__ void acquire_all() {
  if (threadIdx.x == 0) { __builtin_amdgcn_fence(__ATOMIC_ACQUIRE, "agent"); asm volatile("s_waitcnt vmcnt(0)" ::: "memory"); }
  __syncthreads();
}
__device__ __forceinline__ void signal_cnt(char* ws, int idx) {
  asm volatile("s_waitcnt vmcnt(0)" ::: "memory");
  __syncthreads();
  if (threadIdx.x == 0) __hip_atomic_fetch_add(cnt_ptr(ws, idx), 1u, RLX_AGENT);
}

__device__ __forceinline__ void xattn_task(char* ws, rsrc_t rs, int seg, int head, int tid) {
  const int lane = tid & 63, li = lane & 15, g = lane >> 4;
  const int unit = seg * NH + head, rb = seg >> 2;
  const unsigned qf = (unsigned)OFF_QF + unit * 8192, kf = (unsigned)OFF_KF + unit * 8192, vf = (unsigned)OFF_VF + unit * 8192;
  const unsigned km = (unsigned)OFF_KMH + unit * 4096, vm = (unsigned)OFF_VMH + unit * 4096;
  const float scale = 0.08838834764831845f;
  if (threadIdx.x == 0) { wait_cnt(ws, C_Q + rb, 32); wait_cnt(ws, C_KV + rb, 64); }
  __syncthreads();
  f32x4 sc[3][2];
#pragma unroll
  for (int a = 0; a < 3; ++a) { sc[a][0] = (f32x4){0, 0, 0, 0}; sc[a][1] = (f32x4){0, 0, 0, 0}; }
  bf16x8 q[2][4], vx[8];
#pragma unroll
  for (int kk = 0; kk < 4; ++kk) {
    q[0][kk] = ld16(rs, lane * 16, qf + kk * 1024); q[1][kk] = ld16(rs, lane * 16, qf + 4096 + kk * 1024);
    const bf16x8 k1 = ld16(rs, lane * 16, kf + kk * 1024), k2 = ld16(rs, lane * 16, kf + 4096 + kk * 1024);
    sc[1][0] = mfma32(k1, q[0][kk], sc[1][0]); sc[1][1] = mfma32(k1, q[1][kk], sc[1][1]);
    sc[2][0] = mfma32(k2, q[0][kk], sc[2][0]); sc[2][1] = mfma32(k2, q[1][kk], sc[2][1]);
  }
#pragma unroll
  for (int et = 0; et < 8; ++et) vx[et] = ld16(rs, lane * 16, vf + et * 1024);
  if (threadIdx.x < 64) {
    unsigned spins = 0;
    while (!__all(__hip_atomic_load(cnt_ptr(ws, C_CH + seg) + threadIdx.x, RLX_AGENT) != 0u)) { __builtin_amdgcn_s_sleep(4); if (++spins > (1u << 18)) break; }
  }
  __syncthreads();
  bf16x8 k0[4]; bf16x4 vmf[8];
#pragma unroll
  for (int kk = 0; kk < 4; ++kk) k0[kk] = ld16(rs, lane * 16, km + kk * 1024);
#pragma unroll
  for (int et = 0; et < 8; ++et) vmf[et] = ld8(rs, lane * 8, vm + et * 512);
#pragma unroll
  for (int kk = 0; kk < 4; ++kk) { sc[0][0] = mfma32(k0[kk], q[0][kk], sc[0][0]); sc[0][1] = mfma32(k0[kk], q[1][kk], sc[0][1]); }
  bf16x4 p0[2]; bf16x8 p12[2];
#pragma unroll
  for (int qt = 0; qt < 2; ++qt) {
    const int qi = 16 * qt + li;
    float v[12]; float m = -3.0e38f;
#pragma unroll
    for (int kt = 0; kt < 3; ++kt)
#pragma unroll
      for (int r = 0; r < 4; ++r) {
        float s = bfr(bfr(sc[kt][qt][r]) * scale);
        bool ok = (kt == 0) ? (g < 2) : (16 * (kt - 1) + 4 * g + r <= qi);
        s = ok ? s : -3.0e38f;
        v[kt * 4 + r] = s; m = fmaxf(m, s);
      }
    m = xmax16_32(m);
    float sum = 0.f;
#pragma unroll
    for (int e = 0; e < 12; ++e) { v[e] = (v[e] > -1.0e38f) ? __expf(v[e] - m) : 0.f; sum += v[e]; }
    sum += __shfl_xor(sum, 16); sum += __shfl_xor(sum, 32);
    const float inv = 1.f / sum;
#pragma unroll
    for (int r = 0; r < 4; ++r) { p0[qt][r] = (__bf16)(v[r] * inv); p12[qt][r] = (__bf16)(v[4 + r] * inv); p12[qt][4 + r] = (__bf16)(v[8 + r] * inv); }
  }
#pragma unroll
  for (int kk = 0; kk < 4; ++kk) {
    f32x4 o[2][2];
#pragma unroll
    for (int e2 = 0; e2 < 2; ++e2)
#pragma unroll
      for (int qt = 0; qt < 2; ++qt) {
        o[qt][e2] = mfma16(vmf[2 * kk + e2], p0[qt], (f32x4){0, 0, 0, 0});
        o[qt][e2] = mfma32(vx[2 * kk + e2], p12[qt], o[qt][e2]);
      }
#pragma unroll
    for (int qt = 0; qt < 2; ++qt)
      st16(rs, (unsigned)OFF_OX + ((((seg * 2 + qt) * NH + head) * 4 + kk) * 64 + lane) * 16, cat(pk4(o[qt][0]), pk4(o[qt][1])));
  }
  signal_cnt(ws, C_X + seg);
}

__device__ __forceinline__ void fastkv_task(const bf16* __restrict__ xn, const bf16* __restrict__ Wk, const bf16* __restrict__ Wv,
                                            char* lds, rsrc_t rs, int t, int tid) {
  const int lane = tid & 63, w = tid >> 6, li = lane & 15, g = lane >> 4, q4 = li >> 2, p4 = li & 3;
  const int isv = t >> 6, head = (t >> 1) & 31, hh = t & 1, tt = w >> 2, nt = w & 3;
  const bf16* W = (isv ? Wv : Wk) + head * DH + hh * 64;
  constexpr int P = 144;
#pragma unroll
  for (int u = 0; u < 8; ++u) {
    const int rr = 64 * u + (tid >> 3), ch = tid & 7;
    *(u32x4*)(lds + rr * P + ch * 16) = *(const u32x4*)(W + (size_t)rr * (NH * DH) + ch * 8);
  }
  bf16x8 af[16];
#pragma unroll
  for (int ks = 0; ks < 16; ++ks) af[ks] = *(const bf16x8*)(xn + (size_t)(16 * tt + li) * DM + 32 * ks + 8 * g);
  __syncthreads();
  f32x4 acc = {0, 0, 0, 0};
#pragma unroll
  for (int ks = 0; ks < 16; ++ks) {
    const bf16x8 bfr_ = cat(tr_read(lds, (32 * ks + 8 * g + q4) * P + (16 * nt + 4 * p4) * 2), tr_read(lds, (32 * ks + 8 * g + 4 + q4) * P + (16 * nt + 4 * p4) * 2));
    if (isv) acc = mfma32(af[ks], bfr_, acc); else acc = mfma32(bfr_, af[ks], acc);
  }
  if (isv) st8(rs, (unsigned)OFF_VF + ((head * 8 + hh * 4 + nt) * 64 + lane) * 16 + tt * 8, pk4(acc));
  else st8(rs, (unsigned)OFF_KF + (((head * 2 + tt) * 4 + hh * 2 + (nt >> 1)) * 64 + lane) * 16 + (nt & 1) * 8, pk4(acc));
}

__device__ __forceinline__ void wof_task(const bf16* __restrict__ Wo, char* lds, rsrc_t rs, int t, int tid) {
  const int lane = tid & 63, w = tid >> 6, li = lane & 15, g = lane >> 4, q4 = li >> 2, p4 = li & 3;
  const int h = t >> 1, chh = t & 1;
  constexpr int P = 528;
#pragma unroll
  for (int u = 0; u < 8; ++u) {
    const int cidx = tid + 512 * u, rr = cidx >> 5, ch = cidx & 31;
    *(u32x4*)(lds + rr * P + ch * 16) = *(const u32x4*)(Wo + ((size_t)h * DH + rr) * DM + chh * 256 + ch * 8);
  }
  __syncthreads();
#pragma unroll
  for (int n2 = 0; n2 < 2; ++n2)
#pragma unroll
    for (int kk = 0; kk < 4; ++kk) {
      const int ntl = 2 * w + n2;
      bf16x4 lo = tr_read(lds, ((2 * kk + 0) * 16 + 4 * g + q4) * P + (16 * ntl + 4 * p4) * 2);
      bf16x4 hi = tr_read(lds, ((2 * kk + 1) * 16 + 4 * g + q4) * P + (16 * ntl + 4 * p4) * 2);
      st16(rs, (unsigned)OFF_WOF + ((((chh * 16 + ntl) * NH + h) * 4 + kk) * 64 + lane) * 16, cat(lo, hi));
    }
}

__device__ __forceinline__ void final_task(char* ws, rsrc_t rs, char* lds, const bf16* __restrict__ x, bf16* __restrict__ out, int seg, int cb, int tid) {
  const int lane = tid & 63, w = __builtin_amdgcn_readfirstlane(tid >> 6);
  if (tid == 0) { wait_cnt(ws, C_WOF, 64); wait_cnt(ws, C_BO + (seg >> 2), 4); }
  __syncthreads();
  bf16x8 bf[16][2];
#pragma unroll
  for (int i = 0; i < 16; ++i)
#pragma unroll
    for (int nt = 0; nt < 2; ++nt) bf[i][nt] = ld16(rs, lane * 16, (unsigned)OFF_WOF + ((cb * 2 + nt) * 128 + w * 16 + i) * 1024);
  const int tt_ = (tid >> 7) & 1, nt_ = (tid >> 6) & 1, li = lane & 15, g = lane >> 4;
  const int row = seg * 32 + tt_ * 16 + li, col = cb * 32 + nt_ * 16 + 4 * g;
  const bf16x4 xv = *(const bf16x4*)(x + (size_t)row * DM + col);
  const bf16x4 bv = ld8(rs, (row * DM + col) * 2, (unsigned)OFF_BOUT);
  if (tid == 0) wait_cnt(ws, C_X + seg, 4);
  __syncthreads();
  f32x4 acc[2][2];
#pragma unroll
  for (int a = 0; a < 2; ++a) { acc[a][0] = (f32x4){0.f, 0.f, 0.f, 0.f}; acc[a][1] = (f32x4){0.f, 0.f, 0.f, 0.f}; }
#pragma unroll
  for (int i4 = 0; i4 < 4; ++i4) {
    bf16x8 af[4][2];
#pragma unroll
    for (int i = 0; i < 4; ++i)
#pragma unroll
      for (int tt = 0; tt < 2; ++tt) af[i][tt] = ld16(rs, lane * 16, (unsigned)OFF_OX + ((seg * 2 + tt) * 128 + w * 16 + i4 * 4 + i) * 1024);
#pragma unroll
    for (int i = 0; i < 4; ++i)
#pragma unroll
      for (int tt = 0; tt < 2; ++tt)
#pragma unroll
        for (int nt = 0; nt < 2; ++nt) acc[tt][nt] = mfma32(bf[i4 * 4 + i][nt], af[i][tt], acc[tt][nt]);
    if (i4 == 1) __builtin_amdgcn_sched_barrier(0);
  }
#pragma unroll
  for (int tt = 0; tt < 2; ++tt)
#pragma unroll
    for (int nt = 0; nt < 2; ++nt) *(f32x4*)(lds + (((w * 2 + tt) * 2 + nt) * 64 + lane) * 16) = acc[tt][nt];
  __syncthreads();
  if (tid < 256) {
    f32x4 a = {0, 0, 0, 0};
#pragma unroll
    for (int ww = 0; ww < 8; ++ww) a += *(const f32x4*)(lds + (((ww * 2 + tt_) * 2 + nt_) * 64 + lane) * 16);
    f32x4 o;
#pragma unroll
    for (int r = 0; r < 4; ++r) o[r] = bfr((float)xv[r] + bfr(a[r])) + (float)bv[r];
    *(bf16x4*)(out + (size_t)row * DM + col) = pk4(o);
  }
}

constexpr int T_F = 0, T_KV = 128, T_Q = T_KV + 1024, T_G = T_Q + 512, T_W = T_G + 448, T_B = T_W + 64, T_S = T_B + 64, T_END = T_S + 64 * 20;

__device__ __forceinline__ void worker(const bf16* __restrict__ x, const bf16* __restrict__ Wq, const bf16* __restrict__ Wk, const bf16* __restrict__ Wv,
                                       const bf16* __restrict__ Wo, const bf16* __restrict__ Wg, const bf16* __restrict__ Wu, const bf16* __restrict__ Wd,
                                       bf16* __restrict__ out, char* ws, char* lds) {
  const rsrc_t rs = __builtin_amdgcn_make_buffer_rsrc(ws, 0, WS_SPAN, 0x00020000);
  const bf16* xn = (const bf16*)(ws + OFF_XN);
  volatile int* tk = (volatile int*)(lds + 144 * 1024);
  for (;;) {
    __syncthreads();
    if (threadIdx.x == 0) *tk = (int)__hip_atomic_fetch_add(cnt_ptr(ws, C_TICKET), 1u, RLX_AGENT);
    __syncthreads();
    const int t = *tk;
    int tid = threadIdx.x; asm volatile("" : "+v"(tid));
    if (t >= T_END) break;
    if (t < T_KV) {
      fastkv_task(xn, Wk, Wv, lds, rs, t, tid);
      signal_cnt(ws, C_F);
    } else if (t < T_Q) {
      const int rb = (t - T_KV) >> 6, cb = t & 63;
      if (cb < 32) gemm_tile<0>(xn, DM, Wk, nullptr, NH * DH, DM, rb * 128, cb * 128, lds, rs, (unsigned)OFF_KF, tid);
      else gemm_tile<1>(xn, DM, Wv, nullptr, NH * DH, DM, rb * 128, (cb - 32) * 128, lds, rs, (unsigned)OFF_VF, tid);
      signal_cnt(ws, C_KV + rb);
    } else if (t < T_G) {
      const int u = t - T_Q, rb = u >> 5, cb = u & 31;
      gemm_tile<0>(xn, DM, Wq, nullptr, NH * DH, DM, rb * 128, cb * 128, lds, rs, (unsigned)OFF_QF, tid);
      signal_cnt(ws, C_Q + rb);
    } else if (t < T_W) {
      const int u = t - T_G, rb = u / 28, cb = u - rb * 28;
      gemm_tile<2>(xn, DM, Wg, Wu, FF, DM, rb * 128, cb * 64, lds, rs, (unsigned)OFF_H, tid);
      signal_cnt(ws, C_H + rb);
    } else if (t < T_B) {
      wof_task(Wo, lds, rs, t - T_W, tid);
      signal_cnt(ws, C_WOF);
    } else if (t < T_S) {
      const int u = t - T_B, rb = u >> 2, cb = u & 3;
      if (threadIdx.x == 0) wait_cnt(ws, C_H + rb, 28);
      acquire_all();
      gemm_tile<3>((const bf16*)(ws + OFF_H), FF, Wd, nullptr, DM, FF, rb * 128, cb * 128, lds, rs, (unsigned)OFF_BOUT, tid);
      signal_cnt(ws, C_BO + rb);
    } else {
      const int u = t - T_S, seg = u / 20, k = u - seg * 20;
      if (k < 4) xattn_task(ws, rs, seg, k * 8 + __builtin_amdgcn_readfirstlane(tid >> 6), tid);
      else final_task(ws, rs, lds, x, out, seg, k - 4, tid);
    }
  }
}

constexpr int CL_SCR = 0;
constexpr int CL_KX = 0;
constexpr int CL_VX = CL_KX + 8192;
constexpr int CL_MEM = CL_VX + 4096;
constexpr int CL_QF = CL_MEM + 8192;
constexpr int CL_KF = CL_QF + 4096;
constexpr int CL_OF = CL_KF + 4096;
constexpr int CL_P = CL_OF + 2048;
constexpr int CL_WV = 32768;
constexpr int CL_WO = 98304;
constexpr int CHAIN_LDS = 163840;
constexpr unsigned SENT = 0xFFFFFFFFu;
constexpr unsigned SPIN_CAP = 1u << 18;


__device__ __forceinline__ void chain(const bf16* __restrict__ Wq, const bf16* __restrict__ Wk, const bf16* __restrict__ Wv,
                                      const bf16* __restrict__ Wo, char* ws, char* lds) {
  const int tid = threadIdx.x, lane = tid & 63, w = __builtin_amdgcn_readfirstlane(tid >> 6);
  const int li = lane & 15, g = lane >> 4, q4 = li >> 2, p4 = li & 3;
  const int b = blockIdx.x, h = b >> 1, c = b & 1;
  bf16x8 wqk[2][16];
  {
    constexpr int P1 = 272, P2 = 1040, P3 = 144;
    u32x4 r[8];
    auto ld_qk = [&](int it) {
#pragma unroll
      for (int u = 0; u < 8; ++u) {
        const int cidx = tid + 512 * u, mat = cidx >> 11, rr = (cidx >> 4) & 127, ch = cidx & 15;
        r[u] = *(const u32x4*)((mat ? Wk : Wq) + (size_t)(128 * it + rr) * (NH * DH) + h * DH + ch * 8);
      }
    };
    auto ld_wo = [&]() {
#pragma unroll
      for (int u = 0; u < 8; ++u) {
        const int cidx = tid + 512 * u, rr = cidx >> 6, ch = cidx & 63;
        r[u] = *(const u32x4*)(Wo + ((size_t)h * DH + c * 64 + rr) * DM + ch * 8);
      }
    };
    auto ld_wv = [&]() {
#pragma unroll
      for (int u = 0; u < 8; ++u) {
        const int rr = 64 * u + (tid >> 3), ch = tid & 7;
        r[u] = *(const u32x4*)(Wv + (size_t)rr * (NH * DH) + h * DH + c * 64 + ch * 8);
      }
    };
    char* scr = lds + 32768;
    ld_qk(0);
#pragma unroll
    for (int it = 0; it < 4; ++it) {
#pragma unroll
      for (int u = 0; u < 8; ++u) {
        const int cidx = tid + 512 * u, mat = cidx >> 11, rr = (cidx >> 4) & 127, ch = cidx & 15;
        *(u32x4*)(scr + mat * (128 * P1) + rr * P1 + ch * 16) = r[u];
      }
      __syncthreads();
      if (it < 3) ld_qk(it + 1); else ld_wo();
      const char* base = scr + (w >> 2) * (128 * P1);
#pragma unroll
      for (int k4 = 0; k4 < 4; ++k4)
#pragma unroll
        for (int t = 0; t < 2; ++t) {
          const int c16 = 2 * (w & 3) + t;
          bf16x4 lo = tr_read(base, (32 * k4 + 8 * g + q4) * P1 + (16 * c16 + 4 * p4) * 2);
          bf16x4 hi = tr_read(base, (32 * k4 + 8 * g + 4 + q4) * P1 + (16 * c16 + 4 * p4) * 2);
          wqk[t][4 * it + k4] = cat(lo, hi);
        }
      __syncthreads();
    }
    scr = lds;
#pragma unroll
    for (int u = 0; u < 8; ++u) {
      const int cidx = tid + 512 * u, rr = cidx >> 6, ch = cidx & 63;
      *(u32x4*)(scr + rr * P2 + ch * 16) = r[u];
    }
    __syncthreads();
    ld_wv();
#pragma unroll
    for (int nt = 0; nt < 4; ++nt)
#pragma unroll
      for (int k2 = 0; k2 < 2; ++k2) {
        const int col = 64 * w + 16 * nt + 4 * p4;
        bf16x4 lo = tr_read(scr, ((2 * k2 + 0) * 16 + 4 * g + q4) * P2 + col * 2);
        bf16x4 hi = tr_read(scr, ((2 * k2 + 1) * 16 + 4 * g + q4) * P2 + col * 2);
        *(bf16x8*)(lds + CL_WO + (((w * 4 + nt) * 2 + k2) * 64 + lane) * 16) = cat(lo, hi);
      }
    __syncthreads();
#pragma unroll
    for (int it = 0; it < 4; ++it) {
#pragma unroll
      for (int u = 0; u < 2; ++u) *(u32x4*)(scr + (64 * u + (tid >> 3)) * P3 + (tid & 7) * 16) = r[2 * it + u];
      __syncthreads();
#pragma unroll
      for (int e2 = 0; e2 < 2; ++e2) {
        const int et = 2 * (w & 1) + e2, k4 = w >> 1;
        bf16x4 lo = tr_read(scr, (32 * k4 + 8 * g + q4) * P3 + (16 * et + 4 * p4) * 2);
        bf16x4 hi = tr_read(scr, (32 * k4 + 8 * g + 4 + q4) * P3 + (16 * et + 4 * p4) * 2);
        *(bf16x8*)(lds + CL_WV + ((et * 16 + 4 * it + k4) * 64 + lane) * 16) = cat(lo, hi);
      }
      __syncthreads();
    }
  }
  const auto rs_mem = __builtin_amdgcn_make_buffer_rsrc(ws + OFF_MEMH, 0, 65 * 8192, 0x00020000);
  const auto rs_part = __builtin_amdgcn_make_buffer_rsrc(ws + OFF_PART, 0, 1 << 20, 0x00020000);
  const auto rs_km = __builtin_amdgcn_make_buffer_rsrc(ws + OFF_KMH, 0, 16 << 20, 0x00020000);
  const float scale = 0.08838834764831845f;
  const auto rs_kv = __builtin_amdgcn_make_buffer_rsrc(ws + OFF_KF, 0, 32 << 20, 0x00020000);
  unsigned rdy = 0;
  if (w < 4) {
    unsigned spins = 0;
    while (__builtin_amdgcn_readfirstlane(__hip_atomic_load(cnt_ptr(ws, C_F), RLX_AGENT)) < 128u) { __builtin_amdgcn_s_sleep(4); if (++spins > SPIN_CAP) break; }
    const u32x4 a0 = __builtin_amdgcn_raw_buffer_load_b128(rs_kv, lane * 16, h * 8192 + w * 2048, 16);
    const u32x4 a1 = __builtin_amdgcn_raw_buffer_load_b128(rs_kv, lane * 16, h * 8192 + w * 2048 + 1024, 16);
    const u32x4 a2 = __builtin_amdgcn_raw_buffer_load_b128(rs_kv, lane * 16, (16 << 20) + h * 8192 + c * 4096 + w * 1024, 16);
    *(u32x4*)(lds + lane * 16 + CL_KX + w * 2048) = a0;
    *(u32x4*)(lds + lane * 16 + CL_KX + w * 2048 + 1024) = a1;
    *(u32x4*)(lds + lane * 16 + CL_VX + w * 1024) = a2;
    rdy = 0u;
  }
  const int pst_off = (((8 * w + 2 * (g & 1) + (g >> 1)) * 64 + b) * 8 + li) * 16;
  const char* l16 = lds + lane * 16;
  const char* mfb = lds + CL_MEM + (g * 8 + (li & 7)) * 16;
  const char* wvb = lds + CL_WV + ((w & 3) * 1024 + lane) * 16;
  const char* wob = lds + CL_WO + (w * 512 + lane) * 16;
  for (int s = 0; s < NSEG; ++s) {
    if (w >= 6) {
      u32x4 v[4]; unsigned spins = 0;
      asm volatile("s_waitcnt vmcnt(0)" ::: "memory");
      for (;;) {
        asm volatile("" ::: "memory");
#pragma unroll
        for (int u = 0; u < 4; ++u) v[u] = __builtin_amdgcn_raw_buffer_load_b128(rs_mem, lane * 16, s * 8192 + (w - 6) * 4096 + u * 1024, 16);
        unsigned m = 0;
#pragma unroll
        for (int u = 0; u < 4; ++u) m = max(m, max(v[u][0], v[u][2]));
        if (__all(m != SENT)) break;
        if (++spins > SPIN_CAP) break;
      }
#pragma unroll
      for (int u = 0; u < 4; ++u) *(u32x4*)(l16 + CL_MEM + (w - 6) * 4096 + u * 1024) = v[u];
    }
    __syncthreads();
    if (tid == 256 && s > 0) __hip_atomic_store(cnt_ptr(ws, C_CH + s - 1) + b, 1u, RLX_AGENT);
    u32x4 pf[3];
    const int sn = (s + 1 < NSEG) ? s + 1 : s;
    const int so = (sn * NH + h) * 8192;
    bool early = true;
    if (w < 4) {
      early = __builtin_amdgcn_readfirstlane(rdy) >= 64u;
      pf[0] = __builtin_amdgcn_raw_buffer_load_b128(rs_kv, lane * 16, so + w * 2048, 16);
      pf[1] = __builtin_amdgcn_raw_buffer_load_b128(rs_kv, lane * 16, so + w * 2048 + 1024, 16);
      pf[2] = __builtin_amdgcn_raw_buffer_load_b128(rs_kv, lane * 16, (16 << 20) + so + c * 4096 + w * 1024, 16);
      const int s2 = (s + 2 < NSEG) ? s + 2 : NSEG - 1;
      rdy = __hip_atomic_load(cnt_ptr(ws, C_KV + (s2 >> 2)), RLX_AGENT);
    }
    bf16x8 mf[16];
#pragma unroll
    for (int ks = 0; ks < 16; ++ks) mf[ks] = *(const bf16x8*)(mfb + ks * 512);
    __builtin_amdgcn_sched_barrier(0);
    {
      f32x4 a0 = {0, 0, 0, 0}, a1 = {0, 0, 0, 0};
#pragma unroll
      for (int ks = 0; ks < 16; ++ks) {
        a0 = mfma32(wqk[0][ks], mf[ks], a0);
        a1 = mfma32(wqk[1][ks], mf[ks], a1);
      }
      const bf16x8 o = cat(pk4(a0), pk4(a1));
      *(bf16x8*)(lds + (w < 4 ? CL_QF : CL_KF) + ((w & 3) * 64 + lane) * 16) = o;
      if (w >= 4 && c == 0) __builtin_amdgcn_raw_buffer_store_b128(__builtin_bit_cast(u32x4, o), rs_km, lane * 16, ((s * NH + h) * 4 + (w & 3)) * 1024, 16);
    }
    bf16x4 vm4 = {(__bf16)0.f, (__bf16)0.f, (__bf16)0.f, (__bf16)0.f};
    if (w == 0) {
      bf16x8 kxf[8];
#pragma unroll
      for (int u = 0; u < 8; ++u) kxf[u] = *(const bf16x8*)(l16 + CL_KX + u * 1024);
      __syncthreads();
      f32x4 s0 = {0, 0, 0, 0}, s1 = {0, 0, 0, 0}, s2 = {0, 0, 0, 0};
#pragma unroll
      for (int kk = 0; kk < 4; ++kk) {
        const bf16x8 qf = *(const bf16x8*)(l16 + CL_QF + kk * 1024);
        const bf16x8 k0 = *(const bf16x8*)(l16 + CL_KF + kk * 1024);
        s0 = mfma32(k0, qf, s0); s1 = mfma32(kxf[kk], qf, s1); s2 = mfma32(kxf[4 + kk], qf, s2);
      }
      if (g >= 2) s0 = (f32x4){-3.0e38f, -3.0e38f, -3.0e38f, -3.0e38f};
      float m = fmaxf(fmaxf(fmaxf(s0[0], s0[1]), fmaxf(s0[2], s0[3])), fmaxf(fmaxf(fmaxf(s1[0], s1[1]), fmaxf(s1[2], s1[3])), fmaxf(fmaxf(s2[0], s2[1]), fmaxf(s2[2], s2[3]))));
      m = xmax16_32(m);
      const float cexp = scale * 1.4426950408889634f, mc = -m * cexp;
      bf16x4 p0; bf16x8 p12;
#pragma unroll
      for (int r = 0; r < 4; ++r) {
        p0[r] = (__bf16)__builtin_amdgcn_exp2f(fmaf(s0[r], cexp, mc));
        p12[r] = (__bf16)__builtin_amdgcn_exp2f(fmaf(s1[r], cexp, mc));
        p12[4 + r] = (__bf16)__builtin_amdgcn_exp2f(fmaf(s2[r], cexp, mc));
      }
      *(bf16x8*)(lds + CL_P + lane * 16) = p12;
      *(bf16x4*)(lds + CL_P + 1024 + lane * 8) = p0;
    } else {
      if (w >= 4) {
        f32x4 va = {0, 0, 0, 0}, vb = {0, 0, 0, 0};
        bf16x8 wv[2][4];
#pragma unroll
        for (int k1 = 0; k1 < 4; ++k1) wv[0][k1] = *(const bf16x8*)(wvb + k1 * 1024);
        __syncthreads();
#pragma unroll
        for (int k4 = 0; k4 < 4; ++k4) {
          if (k4 < 3) {
#pragma unroll
            for (int k1 = 0; k1 < 4; ++k1) wv[(k4 + 1) & 1][k1] = *(const bf16x8*)(wvb + (4 * k4 + 4 + k1) * 1024);
          }
#pragma unroll
          for (int k1 = 0; k1 < 4; k1 += 2) { va = mfma32(mf[4 * k4 + k1], wv[k4 & 1][k1], va); vb = mfma32(mf[4 * k4 + k1 + 1], wv[k4 & 1][k1 + 1], vb); }
          __builtin_amdgcn_sched_barrier(0);
        }
        vm4 = pk4(va + vb);
        __builtin_amdgcn_raw_buffer_store_b64(__builtin_bit_cast(u32x2, vm4), rs_km, lane * 8, (8 << 20) + ((s * NH + h) * 8 + 4 * c + (w - 4)) * 512, 16);
      } else {
        __syncthreads();
      }
    }
    bf16x8 wo[4][2];
#pragma unroll
    for (int nt = 0; nt < 4; ++nt)
#pragma unroll
      for (int k2 = 0; k2 < 2; ++k2) wo[nt][k2] = *(const bf16x8*)(wob + (nt * 2 + k2) * 1024);
    __syncthreads();
    if (w >= 4) {
      const bf16x8 p12 = *(const bf16x8*)(lds + CL_P + lane * 16);
      const bf16x4 p0 = *(const bf16x4*)(lds + CL_P + 1024 + lane * 8);
      const bf16x8 vx8 = *(const bf16x8*)(lds + CL_VX + ((w - 4) * 64 + lane) * 16);
      f32x4 o = mfma16(vm4, p0, (f32x4){0, 0, 0, 0});
      const bf16x4 one4 = {(__bf16)1.f, (__bf16)1.f, (__bf16)1.f, (__bf16)1.f};
      f32x4 l = mfma16(one4, p0, (f32x4){0, 0, 0, 0});
      o = mfma32(vx8, p12, o);
      l = mfma32(cat(one4, one4), p12, l);
      const float inv = __builtin_amdgcn_rcpf(l[0]);
      *(bf16x4*)(lds + CL_OF + (((w - 4) >> 1) * 64 + lane) * 16 + (w & 1) * 8) = pk4(o * inv);
    }
    __syncthreads();
    if (s + 1 == NSEG) {
      if (w >= 4) asm volatile("s_waitcnt vmcnt(0)" ::: "memory");
      __syncthreads();
      if (tid == 64) __hip_atomic_store(cnt_ptr(ws, C_CH + s) + b, 1u, RLX_AGENT);
      break;
    }
    const int slot = (s & 1) << 19;
    {
      const bf16x8 of0 = *(const bf16x8*)(l16 + CL_OF);
      const bf16x8 of1 = *(const bf16x8*)(l16 + CL_OF + 1024);
#pragma unroll
      for (int n2 = 0; n2 < 2; ++n2) {
        f32x4 pa = mfma32(wo[2 * n2][0], of0, (f32x4){0, 0, 0, 0});
        f32x4 pb = mfma32(wo[2 * n2 + 1][0], of0, (f32x4){0, 0, 0, 0});
        pa = mfma32(wo[2 * n2][1], of1, pa);
        pb = mfma32(wo[2 * n2 + 1][1], of1, pb);
        u32x2 A = __builtin_bit_cast(u32x2, pk4(pa)), B = __builtin_bit_cast(u32x2, pk4(pb));
        auto r0 = __builtin_amdgcn_permlane16_swap(A[0], B[0], false, false);
        auto r1 = __builtin_amdgcn_permlane16_swap(A[1], B[1], false, false);
        u32x4 pv = {r0[0] | 1u, r1[0], r0[1] | 1u, r1[1]};
        if (li < 8) __builtin_amdgcn_raw_buffer_store_b128(pv, rs_part, pst_off, slot + n2 * 32768, 16);
      }
    }
    if (w < 4) {
      if (!early) {
        unsigned spins = 0;
        while (__builtin_amdgcn_readfirstlane(__hip_atomic_load(cnt_ptr(ws, C_KV + (sn >> 2)), RLX_AGENT)) < 64u) { __builtin_amdgcn_s_sleep(2); if (++spins > SPIN_CAP) break; }
        const u32x4 t0 = __builtin_amdgcn_raw_buffer_load_b128(rs_kv, lane * 16, so + w * 2048, 16);
        const u32x4 t1 = __builtin_amdgcn_raw_buffer_load_b128(rs_kv, lane * 16, so + w * 2048 + 1024, 16);
        const u32x4 t2 = __builtin_amdgcn_raw_buffer_load_b128(rs_kv, lane * 16, (16 << 20) + so + c * 4096 + w * 1024, 16);
        *(u32x4*)(l16 + CL_KX + w * 2048) = t0;
        *(u32x4*)(l16 + CL_KX + w * 2048 + 1024) = t1;
        *(u32x4*)(l16 + CL_VX + w * 1024) = t2;
      } else {
        *(u32x4*)(l16 + CL_KX + w * 2048) = pf[0];
        *(u32x4*)(l16 + CL_KX + w * 2048 + 1024) = pf[1];
        *(u32x4*)(l16 + CL_VX + w * 1024) = pf[2];
      }
    }
    if (w == 4 || w == 5) asm volatile("s_waitcnt vmcnt(0)" ::: "memory");
    if (w >= 6) {
      u32x2 v[8]; unsigned spins = 0;
      const int roff = lane * 16 + (w - 6) * 8;
      asm volatile("s_waitcnt vmcnt(0)" ::: "memory");
      for (;;) {
        asm volatile("" ::: "memory");
#pragma unroll
        for (int it = 0; it < 8; ++it) v[it] = __builtin_amdgcn_raw_buffer_load_b64(rs_part, roff, slot + b * 8192 + it * 1024, 16);
        unsigned m = 1u;
#pragma unroll
        for (int it = 0; it < 8; ++it) m &= v[it][0];
        if (__all(m != 0u)) break;
        if (++spins > SPIN_CAP) break;
      }
      f32x4 a = {0, 0, 0, 0};
#pragma unroll
      for (int it = 0; it < 8; ++it) {
        a[0] += __builtin_bit_cast(float, v[it][0] << 16); a[1] += __builtin_bit_cast(float, v[it][0] & 0xffff0000u);
        a[2] += __builtin_bit_cast(float, v[it][1] << 16); a[3] += __builtin_bit_cast(float, v[it][1] & 0xffff0000u);
      }
#pragma unroll
      for (int r = 0; r < 4; ++r) a[r] = xsum8_16_32(a[r]);
      u32x2 o = __builtin_bit_cast(u32x2, pk4(a));
      if (o[0] == SENT) o[0] = 0x7FFF7FFFu;
      if (lane < 8) __builtin_amdgcn_raw_buffer_store_b64(o, rs_mem, lane * 16 + (w - 6) * 8, (s + 1) * 8192 + b * 128, 16);
      const u32x2 z = {0u, 0u};
#pragma unroll
      for (int it = 0; it < 8; ++it) __builtin_amdgcn_raw_buffer_store_b64(z, rs_part, roff, slot + b * 8192 + it * 1024, 16);
    }
  }
}


__global__ __launch_bounds__(512, 2) void k_main(const bf16* __restrict__ x, const bf16* __restrict__ Wq, const bf16* __restrict__ Wk,
                                                 const bf16* __restrict__ Wv, const bf16* __restrict__ Wo, const bf16* __restrict__ Wg,
                                                 const bf16* __restrict__ Wu, const bf16* __restrict__ Wd, bf16* __restrict__ out, char* ws) {
  extern __shared__ __attribute__((aligned(16))) char lds[];
  if (blockIdx.x < 64) chain(Wq, Wk, Wv, Wo, ws, lds);
  else worker(x, Wq, Wk, Wv, Wo, Wg, Wu, Wd, out, ws, lds);
}

extern "C" void kernel_launch(void* const* d_in, const int* in_sizes, int n_in,
                              void* d_out, int out_size, void* d_ws, size_t ws_size,
                              hipStream_t stream) {
  const bf16* x = (const bf16*)d_in[0];
  const bf16* mem0 = (const bf16*)d_in[1];
  const bf16* Wq = (const bf16*)d_in[2];
  const bf16* Wk = (const bf16*)d_in[3];
  const bf16* Wv = (const bf16*)d_in[4];
  const bf16* Wo = (const bf16*)d_in[5];
  const bf16* Wg = (const bf16*)d_in[8];
  const bf16* Wu = (const bf16*)d_in[9];
  const bf16* Wd = (const bf16*)d_in[10];
  char* ws = (char*)d_ws;
  (void)hipFuncSetAttribute((const void*)k_main, hipFuncAttributeMaxDynamicSharedMemorySize, CHAIN_LDS);
  k_ln<<<S_ / 4, 256, 0, stream>>>(x, mem0, ws);
  k_main<<<256, 512, CHAIN_LDS, stream>>>(x, Wq, Wk, Wv, Wo, Wg, Wu, Wd, (bf16*)d_out, ws);
}
```
